```python
import torch

def _softmax(x, axis=-1):
    return torch.softmax(x, dim=axis)

def _gelu(x):
    return torch.nn.functional.gelu(x, approximate='tanh')

def _silu(x):
    return torch.nn.functional.silu(x)

def _sigmoid(x):
    return torch.sigmoid(x)

def _layernorm(x, axis=-1, eps=1e-05):
    m = x.mean(dim=axis, keepdim=True)
    v = x.var(dim=axis, keepdim=True, unbiased=False)
    return (x - m) / torch.sqrt(v + eps)

def _scatter_add(tgt, idx, src, axis=0):
    return tgt.clone().index_add_(axis, idx, src)

def _segment_sum(src, seg, n):
    out = torch.zeros((n,) + src.shape[1:], dtype=src.dtype)
    return out.index_add_(0, seg, src)

def _pad(x, pw):
    flat = tuple((v for pair in reversed(pw) for v in pair))
    return torch.nn.functional.pad(x, flat)
kernel_name = 'adaLN_cond__softmax_full_mha__dense_activation_silu__prompt'

def setup_inputs():

    _w = lambda a: (lambda t: t.to(torch.bfloat16) if t.is_floating_point() else t)(torch.as_tensor(a))

    def _A_setup():
        B, S, D = (1, 2048, 448)
        return {'x': _w((lambda *s: torch.randn(*s, dtype=torch.bfloat16) * 0.05)(B, S, D)), 'wqkv': _w((lambda *s: torch.randn(*s, dtype=torch.bfloat16) * 0.05)(D, 3 * D)), 'wo': _w((lambda *s: torch.randn(*s, dtype=torch.bfloat16) * 0.05)(D, D))}
    _A_in = _A_setup()

    def _B_setup():
        B, S, D, F = (1, 2048, 448, 1792)
        return {'x': _w((lambda *s: torch.randn(*s, dtype=torch.bfloat16) * 0.05)(B, S, D)), 'w1': _w((lambda *s: torch.randn(*s, dtype=torch.bfloat16) * 3.2)(D, F) * D ** (-0.5)), 'w2': _w((lambda *s: torch.randn(*s, dtype=torch.bfloat16) * 0.2)(F, D) * F ** (-0.5))}
    _B_in = _B_setup()
    return {'A_x': _A_in['x'], 'A_wqkv': _A_in['wqkv'], 'A_wo': _A_in['wo'], 'B_x': _B_in['x'], 'B_w1': _B_in['w1'], 'B_w2': _B_in['w2'], 'c': _w((lambda *s: torch.randn(*s, dtype=torch.bfloat16) * 0.1)(1, 448) * 8.0), 'w_mod': _w((lambda *s: torch.randn(*s, dtype=torch.bfloat16) * 0.1)(448, 6 * 448) * 448 ** (-0.5) * 8.0)}

def reference(A_x, A_wqkv, A_wo, B_x, B_w1, B_w2, c, w_mod):

    def _A_ref(x, wqkv, wo):
        B, S, D = x.shape
        H = 16
        Dh = D // H
        qkv = (lambda x, *d: x.permute(*d))((x @ wqkv).reshape(B, S, 3, H, Dh), 2, 0, 3, 1, 4)
        q, k, v = (qkv[0], qkv[1], qkv[2])
        scale = Dh ** (-0.5)
        scores = torch.einsum('bhqd,bhkd->bhqk', q, k) * scale
        p = _softmax(scores, axis=-1)
        o = torch.einsum('bhqk,bhkd->bhqd', p, v)
        return (lambda x, *d: x.permute(*d))(o, 0, 2, 1, 3).reshape(B, S, D) @ wo

    def _B_ref(x, w1, w2):
        h = torch.einsum('bsd,df->bsf', x, w1)
        h = _silu(h)
        out = torch.einsum('bsf,fd->bsd', h, w2)
        return out
    x = A_x
    mod = (_silu(c) @ w_mod).reshape(c.shape[0], 6, 1, -1)
    sa, ca, ga, sb, cb, gb = (mod[:, 0].clone(), mod[:, 1].clone(), mod[:, 2].clone(), mod[:, 3].clone(), mod[:, 4].clone(), mod[:, 5].clone())
    h = _layernorm(x, axis=-1) * (1 + ca) + sa
    x = x + ga * _A_ref(h, A_wqkv, A_wo)
    h = _layernorm(x, axis=-1) * (1 + cb) + sb
    x = x + gb * _B_ref(h, B_w1, B_w2)
    return x
```

```cpp
#include <hip/hip_runtime.h>
#include <hip/hip_bf16.h>
#include <stdint.h>

typedef unsigned short u16;
using bf16x8 = __attribute__((ext_vector_type(8))) short;
using s16x4 = __attribute__((ext_vector_type(4))) short;
using f32x16 = __attribute__((ext_vector_type(16))) float;
using f32x4 = __attribute__((ext_vector_type(4))) float;
using f32x2 = __attribute__((ext_vector_type(2))) float;
using u32x4 = __attribute__((ext_vector_type(4))) unsigned;
using u32x2 = __attribute__((ext_vector_type(2))) unsigned;
typedef short v4i16_t __attribute__((ext_vector_type(4)));
#define LAS __attribute__((address_space(3)))
typedef LAS char* lds_ptr;
#define DI __device__ __forceinline__
#define MFMA32(a, b, c) __builtin_amdgcn_mfma_f32_32x32x16_bf16((a), (b), (c), 0, 0, 0)

constexpr int S = 2048, D = 448, NH = 16, DH = 28, F = 1792, NQKV = 3 * D, NMOD = 6 * D;
constexpr float LN_EPS = 1e-5f;
constexpr float QSCALE = 0.18898223650461362f * 1.4426950408889634f;

constexpr size_t WS_MOD = 0;
constexpr size_t WS_ST1 = 16384;
constexpr size_t WS_ST2 = WS_ST1 + 2048 * 8;
constexpr size_t WS_Q = WS_ST2 + 2048 * 16 * 8;
constexpr size_t QKV_BYTES = (size_t)NH * 64 * 2048;
constexpr size_t WS_K = WS_Q + QKV_BYTES;
constexpr size_t WS_V = WS_K + QKV_BYTES;
constexpr size_t WS_O = WS_V + QKV_BYTES;
constexpr size_t WS_X1 = WS_O + (size_t)S * D * 2;
constexpr size_t WS_H = WS_X1 + (size_t)S * D * 2;
constexpr size_t WS_END = WS_H + (size_t)S * F * 2;

DI float bf2f(u16 v) { return __uint_as_float((unsigned)v << 16); }
DI float bflo(unsigned w) { return __uint_as_float(w << 16); }
DI float bfhi(unsigned w) { return __uint_as_float(w & 0xffff0000u); }
DI unsigned cvtpk(float lo, float hi) {
  typedef __bf16 bf2_t __attribute__((ext_vector_type(2)));
  bf2_t r = __builtin_convertvector((f32x2){lo, hi}, bf2_t);
  return __builtin_bit_cast(unsigned, r);
}
DI float bfround(float x) { return bflo(cvtpk(x, 0.f)); }
DI void glds16(const void* sbase, unsigned voff, unsigned lds_base) {
  unsigned sv;
  asm volatile("s_mov_b32 %0, m0\n\ts_mov_b32 m0, %3\n\ts_nop 0\n\tglobal_load_lds_dwordx4 %1, %2\n\ts_mov_b32 m0, %0"
               : "=&s"(sv) : "v"(voff), "s"(sbase), "s"(lds_base) : "memory");
}
#define WAITV(N) asm volatile("s_waitcnt vmcnt(" #N ")" ::: "memory")
#define BAR() asm volatile("s_waitcnt lgkmcnt(0)\n\ts_barrier" ::: "memory")
template <int N> DI void waitv() {
  static_assert(N >= 0 && N <= 63, "vmcnt");
  asm volatile("s_waitcnt vmcnt(%0)" ::"i"(N) : "memory");
}
DI s16x4 trread(lds_ptr p) { return __builtin_bit_cast(s16x4, __builtin_amdgcn_ds_read_tr16_b64_v4i16((LAS v4i16_t*)p)); }
DI bf16x8 cat8(s16x4 a, s16x4 b) { return __builtin_shufflevector(a, b, 0, 1, 2, 3, 4, 5, 6, 7); }
DI bf16x8 pack8(const f32x16& x, int s) {
  u32x4 w = {cvtpk(x[8 * s], x[8 * s + 1]), cvtpk(x[8 * s + 2], x[8 * s + 3]), cvtpk(x[8 * s + 4], x[8 * s + 5]), cvtpk(x[8 * s + 6], x[8 * s + 7])};
  return __builtin_bit_cast(bf16x8, w);
}
DI float swap32_max(float m) {
  auto rr = __builtin_amdgcn_permlane32_swap(__float_as_uint(m), __float_as_uint(m), false, false);
  return fmaxf(__uint_as_float(rr[0]), __uint_as_float(rr[1]));
}
DI float swap32_add(float m) {
  auto rr = __builtin_amdgcn_permlane32_swap(__float_as_uint(m), __float_as_uint(m), false, false);
  return __uint_as_float(rr[0]) + __uint_as_float(rr[1]);
}

constexpr int K0_MODB = NMOD / 32, K0_STB = S / 16;
__global__ __launch_bounds__(256) void k0_mod_stats(const u16* __restrict__ x, const u16* __restrict__ c, const u16* __restrict__ wmod,
                                                    float* __restrict__ mod, f32x2* __restrict__ st1) {
  __shared__ float red[4][32];
  const int tid = threadIdx.x, lane = tid & 63, wid = tid >> 6;
  if (blockIdx.x < K0_MODB) {
    const int cg = tid & 3, rg = tid >> 2, n0 = blockIdx.x * 32 + cg * 8;
    u32x4 w[7]; float sc[7];
#pragma unroll
    for (int i = 0; i < 7; ++i) w[i] = *(const u32x4*)(wmod + (size_t)(rg * 7 + i) * NMOD + n0);
#pragma unroll
    for (int i = 0; i < 7; ++i) { float cv = bf2f(c[rg * 7 + i]); sc[i] = bfround(cv / (1.f + __expf(-cv))); }
    float acc[8] = {0, 0, 0, 0, 0, 0, 0, 0};
#pragma unroll
    for (int i = 0; i < 7; ++i)
#pragma unroll
      for (int j = 0; j < 4; ++j) { acc[2 * j] += sc[i] * bflo(w[i][j]); acc[2 * j + 1] += sc[i] * bfhi(w[i][j]); }
#pragma unroll
    for (int j = 0; j < 8; ++j) {
      float v = acc[j];
      v += __shfl_xor(v, 4); v += __shfl_xor(v, 8); v += __shfl_xor(v, 16); v += __shfl_xor(v, 32);
      acc[j] = v;
    }
    if (lane < 4) {
#pragma unroll
      for (int j = 0; j < 8; ++j) red[wid][lane * 8 + j] = acc[j];
    }
    __syncthreads();
    if (tid < 32) {
      float v = bfround(red[0][tid] + red[1][tid] + red[2][tid] + red[3][tid]);
      const int n = blockIdx.x * 32 + tid, which = n / D;
      if (which == 1 || which == 4) v = bfround(1.f + v);
      mod[n] = v;
    }
  } else {
    const int r0 = (blockIdx.x - K0_MODB) * 16 + wid * 4;
    u32x4 w[4];
#pragma unroll
    for (int i = 0; i < 4; ++i) w[i] = lane < 56 ? *(const u32x4*)(x + (size_t)(r0 + i) * D + lane * 8) : (u32x4){0, 0, 0, 0};
#pragma unroll
    for (int i = 0; i < 4; ++i) {
      float s = 0;
#pragma unroll
      for (int j = 0; j < 4; ++j) s += bflo(w[i][j]) + bfhi(w[i][j]);
#pragma unroll
      for (int o = 1; o < 64; o <<= 1) s += __shfl_xor(s, o);
      const float mean = s * (1.f / D);
      float q = 0;
#pragma unroll
      for (int j = 0; j < 4; ++j) { float a = bflo(w[i][j]) - mean, b = bfhi(w[i][j]) - mean; q += a * a + b * b; }
      if (lane >= 56) q = 0;
#pragma unroll
      for (int o = 1; o < 64; o <<= 1) q += __shfl_xor(q, o);
      const float rstd = 1.f / sqrtf(q * (1.f / D) + LN_EPS);
      if (lane == 0) st1[r0 + i] = (f32x2){rstd, -mean * rstd};
    }
  }
}

DI unsigned a_frag_off(int lane, int s) { const int r = lane & 31, h = lane >> 5; return r * 128 + ((((2 * s + h) ^ ((r >> 1) & 7))) << 4); }
template <int NC> DI unsigned b_frag_off(int lane, int cu) {
  const int h = lane >> 5, g1 = (lane >> 4) & 1, q = (lane >> 2) & 3, p = lane & 3, col = cu + 16 * g1 + 4 * p;
  return 2 * h * (NC * 64) + (col >> 3) * 64 + q * 16 + ((col >> 2) & 1) * 8;
}

DI void ln_chunk(lds_ptr p, float rstd, float nmr, const LAS float* av, const LAS float* bv) {
  u32x4 w = *(LAS u32x4*)p;
  const f32x4 a0 = *(const LAS f32x4*)av, a1 = *(const LAS f32x4*)(av + 4), b0 = *(const LAS f32x4*)bv, b1 = *(const LAS f32x4*)(bv + 4);
  u32x4 o;
  o[0] = cvtpk(fmaf(fmaf(bflo(w[0]), rstd, nmr), a0[0], b0[0]), fmaf(fmaf(bfhi(w[0]), rstd, nmr), a0[1], b0[1]));
  o[1] = cvtpk(fmaf(fmaf(bflo(w[1]), rstd, nmr), a0[2], b0[2]), fmaf(fmaf(bfhi(w[1]), rstd, nmr), a0[3], b0[3]));
  o[2] = cvtpk(fmaf(fmaf(bflo(w[2]), rstd, nmr), a1[0], b1[0]), fmaf(fmaf(bfhi(w[2]), rstd, nmr), a1[1], b1[1]));
  o[3] = cvtpk(fmaf(fmaf(bflo(w[3]), rstd, nmr), a1[2], b1[2]), fmaf(fmaf(bfhi(w[3]), rstd, nmr), a1[3], b1[3]));
  *(LAS u32x4*)p = o;
}

constexpr int K1_NC = 21, K1_BSLOT = 64 * K1_NC * 16 + 512, K1_NSLOT = 4;
constexpr int K1_A = 0, K1_B = 7 * 8192, K1_AB = K1_B + K1_NSLOT * K1_BSLOT, K1_LDS = K1_AB + 5120;
__global__ __launch_bounds__(512) void k1_qkv(const u16* __restrict__ x, const u16* __restrict__ w, const float* __restrict__ mod,
                                              const f32x2* __restrict__ st1, u16* __restrict__ qo, u16* __restrict__ ko, u16* __restrict__ vo) {
  extern __shared__ __attribute__((aligned(16))) char smem[];
  lds_ptr sm = (lds_ptr)smem;
  const unsigned lds0 = (unsigned)(uintptr_t)smem;
  const int tid = threadIdx.x, lane = tid & 63, wid = __builtin_amdgcn_readfirstlane(tid >> 6);
  const int cb = blockIdx.x & 7, rb = blockIdx.x >> 3;
  const int row0 = rb * 64;
  const int arow = wid * 8 + (lane >> 3), aslot = lane & 7, achunk = aslot ^ ((arow >> 1) & 7);
  const unsigned aoff = (unsigned)((row0 + arow) * (D * 2) + achunk * 16);
  unsigned boff[3];
#pragma unroll
  for (int i = 0; i < 3; ++i) {
    const int U = (wid + 7 * i) * 16 + (lane >> 2), kg = U / K1_NC, ch = U % K1_NC;
    boff[i] = (unsigned)((4 * kg + (lane & 3)) * (NQKV * 2) + cb * (K1_NC * 16) + ch * 16);
  }
  auto issueB = [&](int st) {
    if (wid < 7) {
      const char* wb = (const char*)w + (size_t)st * 64 * (NQKV * 2);
      const unsigned dst = lds0 + K1_B + (st % K1_NSLOT) * K1_BSLOT + wid * 1024;
#pragma unroll
      for (int i = 0; i < 3; ++i) glds16(wb, boff[i], dst + i * 7 * 1024);
    }
  };
  auto issueA = [&](int st) { glds16((const char*)x + st * 128, aoff, lds0 + K1_A + st * 8192 + wid * 1024); };
  if (wid == 7) {
#pragma unroll
    for (int i = 0; i < 4; ++i) glds16(mod, lane * 16 + i * 1024, lds0 + K1_AB + i * 1024);
    glds16(st1 + row0, lane * 16, lds0 + K1_AB + 4096);
  }
  issueA(0); issueB(0); issueA(1); issueB(1); issueA(2); issueB(2); issueA(3); issueA(4); issueA(5); issueA(6);
  if (wid == 7) waitv<7>();
  BAR();
  const LAS float* bv = (const LAS float*)(sm + K1_AB) + achunk * 8;
  const LAS float* av = bv + D;
  const f32x2 st = *(const LAS f32x2*)(sm + K1_AB + 4096 + arow * 8);
  const float rstd = st[0], nmr = st[1];
  lds_ptr mychunk = sm + K1_A + wid * 1024 + lane * 16;

  const int rt = wid & 1, ug = (wid >> 1) & 1;
  f32x16 acc[3] = {};
  unsigned afo[4], bfo[3];
#pragma unroll
  for (int s = 0; s < 4; ++s) afo[s] = K1_A + rt * 4096 + a_frag_off(lane, s);
#pragma unroll
  for (int u = 0; u < 3; ++u) bfo[u] = K1_B + b_frag_off<K1_NC>(lane, (ug * 3 + u) * DH);
  const int unit0 = cb * 6 + ug * 3;

#pragma unroll
  for (int stg = 0; stg < 7; ++stg) {
    if (stg == 0) waitv<12>();
    else if (stg == 1) waitv<8 + 3>();
    else if (stg == 2) waitv<4 + 6>();
    else if (stg == 3) waitv<6>();
    else if (stg == 4) waitv<6>();
    else if (stg == 5) waitv<3>();
    else waitv<0>();
    if (wid == 7) {
      if (stg == 0) waitv<6>(); else if (stg == 1) waitv<5>(); else if (stg == 2) waitv<4>(); else if (stg == 3) waitv<3>();
      else if (stg == 4) waitv<2>(); else if (stg == 5) waitv<1>();
    }
    ln_chunk(mychunk + stg * 8192, rstd, nmr, av + stg * 64, bv + stg * 64);
    BAR();
    if (stg + 3 < 7) issueB(stg + 3);
    if (wid < 4) {
      const unsigned bs = (stg % K1_NSLOT) * K1_BSLOT;
#pragma unroll
      for (int s = 0; s < 4; ++s) {
        const bf16x8 a = *(const LAS bf16x8*)(sm + afo[s] + stg * 8192);
#pragma unroll
        for (int u = 0; u < 3; ++u) {
          const bf16x8 b = cat8(trread(sm + bfo[u] + bs + (4 * s) * (K1_NC * 64)), trread(sm + bfo[u] + bs + (4 * s + 1) * (K1_NC * 64)));
          if (unit0 + u < 32) acc[u] = MFMA32(b, a, acc[u]);
          else acc[u] = MFMA32(a, b, acc[u]);
        }
      }
    }
  }
  if (wid < 4) {
    const int h = lane >> 5, tile = rb * 2 + rt;
#pragma unroll
    for (int u = 0; u < 3; ++u) {
      const int unit = unit0 + u, typ = unit >> 4, head = unit & 15;
      f32x16 v = acc[u];
      u16* dst;
      if (typ == 0) {
#pragma unroll
        for (int r = 0; r < 16; ++r) v[r] *= QSCALE;
        if (h) { v[12] = 0; v[13] = 0; v[14] = 0; v[15] = 0; }
        dst = qo;
      } else if (typ == 1) {
        if (h) { v[12] = 1.f; v[13] = 0; v[14] = 0; v[15] = 0; }
        dst = ko;
      } else {
        const int d = lane & 31;
        if (d >= DH) {
          const float pv = d == DH ? 1.f : 0.f;
#pragma unroll
          for (int r = 0; r < 16; ++r) v[r] = pv;
        }
        dst = vo;
      }
      u16* p = dst + ((size_t)(head * 64 + tile) * 2) * 512 + lane * 8;
      *(bf16x8*)p = pack8(v, 0);
      *(bf16x8*)(p + 512) = pack8(v, 1);
    }
  }
}

constexpr int K2_LDS = 8 * 8192 + 8 * 2 * 32 * 8;
__global__ __launch_bounds__(512) void k2_attn(const u16* __restrict__ qb, const u16* __restrict__ kb, const u16* __restrict__ vb, u16* __restrict__ ob) {
  extern __shared__ __attribute__((aligned(16))) char smem[];
  lds_ptr sm = (lds_ptr)smem;
  const int tid = threadIdx.x, lane = tid & 63, wid = __builtin_amdgcn_readfirstlane(tid >> 6), h = lane >> 5;
  const int head = (blockIdx.x & 7) * 2 + ((blockIdx.x >> 3) & 1), qblk = blockIdx.x >> 4;
  const int qh = wid & 1, kq = wid >> 1;
  const u16* qp = qb + ((size_t)(head * 64 + qblk * 4 + qh * 2) * 2) * 512 + lane * 8;
  const u16* kp = kb + ((size_t)(head * 64 + kq * 16) * 2) * 512 + lane * 8;
  const u16* vp = vb + ((size_t)(head * 64 + kq * 16) * 2) * 512 + lane * 8;
  bf16x8 qf[2][2];
#pragma unroll
  for (int i = 0; i < 2; ++i) { qf[i][0] = *(const bf16x8*)(qp + i * 1024); qf[i][1] = *(const bf16x8*)(qp + i * 1024 + 512); }
  bf16x8 kc0 = *(const bf16x8*)kp, kc1 = *(const bf16x8*)(kp + 512), vc0 = *(const bf16x8*)vp, vc1 = *(const bf16x8*)(vp + 512);
  bf16x8 kn0 = *(const bf16x8*)(kp + 1024), kn1 = *(const bf16x8*)(kp + 1536), vn0 = *(const bf16x8*)(vp + 1024), vn1 = *(const bf16x8*)(vp + 1536);
  const f32x16 zero = {};
  float mref[2];
  bf16x8 qs[2];
#pragma unroll
  for (int i = 0; i < 2; ++i) {
    f32x16 s = MFMA32(kc0, qf[i][0], zero); s = MFMA32(kc1, qf[i][1], s);
    float m = s[0];
#pragma unroll
    for (int r = 1; r < 16; ++r) m = fmaxf(m, s[r]);
    m = swap32_max(m);
    const unsigned mb = cvtpk(-m, 0.f);
    mref[i] = -bflo(mb);
    u32x4 t = __builtin_bit_cast(u32x4, qf[i][1]);
    if (h) t[2] = mb;
    qs[i] = __builtin_bit_cast(bf16x8, t);
  }
  f32x16 o[2] = {};
#pragma unroll 2
  for (int kt = 0; kt < 16; ++kt) {
    const int nx = kt + 2 < 16 ? kt + 2 : 15;
    const bf16x8 kf0 = *(const bf16x8*)(kp + nx * 1024), kf1 = *(const bf16x8*)(kp + nx * 1024 + 512);
    const bf16x8 vf0 = *(const bf16x8*)(vp + nx * 1024), vf1 = *(const bf16x8*)(vp + nx * 1024 + 512);
#pragma unroll
    for (int i = 0; i < 2; ++i) {
      f32x16 s = MFMA32(kc0, qf[i][0], zero); s = MFMA32(kc1, qs[i], s);
#pragma unroll
      for (int r = 0; r < 16; ++r) s[r] = __builtin_amdgcn_exp2f(s[r]);
      o[i] = MFMA32(vc0, pack8(s, 0), o[i]); o[i] = MFMA32(vc1, pack8(s, 1), o[i]);
    }
    kc0 = kn0; kc1 = kn1; vc0 = vn0; vc1 = vn1; kn0 = kf0; kn1 = kf1; vn0 = vf0; vn1 = vf1;
  }
  bool bad = false;
#pragma unroll
  for (int i = 0; i < 2; ++i) {
    float tot = 0.f;
#pragma unroll
    for (int r = 0; r < 16; ++r) tot += o[i][r] * 0.f;
    bad |= !(tot == 0.f) || (h && !(o[i][12] < 1.8e19f));
  }
  if (__any(bad)) {
#pragma unroll
    for (int i = 0; i < 2; ++i) { o[i] = zero; mref[i] = -INFINITY; }
    for (int kt = 0; kt < 16; ++kt) {
      const bf16x8 k0 = *(const bf16x8*)(kp + kt * 1024), k1 = *(const bf16x8*)(kp + kt * 1024 + 512);
      const bf16x8 v0 = *(const bf16x8*)(vp + kt * 1024), v1 = *(const bf16x8*)(vp + kt * 1024 + 512);
#pragma unroll
      for (int i = 0; i < 2; ++i) {
        f32x16 s = MFMA32(k0, qf[i][0], zero); s = MFMA32(k1, qf[i][1], s);
        float m = s[0];
#pragma unroll
        for (int r = 1; r < 16; ++r) m = fmaxf(m, s[r]);
        m = fmaxf(swap32_max(m), mref[i]);
        const float al = __builtin_amdgcn_exp2f(mref[i] - m);
        mref[i] = m;
#pragma unroll
        for (int r = 0; r < 16; ++r) { s[r] = __builtin_amdgcn_exp2f(s[r] - m); o[i][r] *= al; }
        o[i] = MFMA32(v0, pack8(s, 0), o[i]); o[i] = MFMA32(v1, pack8(s, 1), o[i]);
      }
    }
  }
  LAS f32x2* ml = (LAS f32x2*)(sm + 8 * 8192);
#pragma unroll
  for (int i = 0; i < 2; ++i) {
#pragma unroll
    for (int g = 0; g < 4; ++g)
      *(LAS f32x4*)(sm + wid * 8192 + (i * 4 + g) * 1024 + lane * 16) = (f32x4){o[i][4 * g], o[i][4 * g + 1], o[i][4 * g + 2], o[i][4 * g + 3]};
    if (h) ml[(wid * 2 + i) * 32 + (lane & 31)] = (f32x2){mref[i], o[i][12]};
  }
  __syncthreads();
  {
    const int i = kq >> 1, q = lane & 31;
    f32x2 e[4];
#pragma unroll
    for (int j = 0; j < 4; ++j) e[j] = ml[((j * 2 + qh) * 2 + i) * 32 + q];
    const float mm = fmaxf(fmaxf(e[0][0], e[1][0]), fmaxf(e[2][0], e[3][0]));
    float f[4], l = 0;
#pragma unroll
    for (int j = 0; j < 4; ++j) { f[j] = __builtin_amdgcn_exp2f(e[j][0] - mm); l += f[j] * e[j][1]; }
    const float rl = 1.f / l;
    u16* orow = ob + (size_t)(qblk * 128 + qh * 64 + i * 32 + q) * D + head * DH;
#pragma unroll
    for (int gg = 0; gg < 2; ++gg) {
      const int g = (kq & 1) * 2 + gg;
      f32x4 a = {0, 0, 0, 0};
#pragma unroll
      for (int j = 0; j < 4; ++j) a += *(const LAS f32x4*)(sm + (j * 2 + qh) * 8192 + (i * 4 + g) * 1024 + lane * 16) * f[j];
      a *= rl;
      if (!(g == 3 && h)) *(u32x2*)(orow + 8 * g + 4 * h) = (u32x2){cvtpk(a[0], a[1]), cvtpk(a[2], a[3])};
    }
  }
}

template <int KT, bool STATS>
__global__ __launch_bounds__(512) void k_proj(const u16* __restrict__ a, const u16* __restrict__ w, const u16* __restrict__ res,
                                              const float* __restrict__ gate, u16* __restrict__ out, f32x2* __restrict__ st2) {
  constexpr int NST = KT / 64, NSLOT = NST < 8 ? NST : 8, PRE = NSLOT < NST ? NSLOT - 1 : NST;
  extern __shared__ __attribute__((aligned(16))) char smem[];
  lds_ptr sm = (lds_ptr)smem;
  const unsigned lds0 = (unsigned)(uintptr_t)smem;
  const int tid = threadIdx.x, lane = tid & 63, wid = __builtin_amdgcn_readfirstlane(tid >> 6);
  const int xg = blockIdx.x & 7, idx = blockIdx.x >> 3, rb = xg * 4 + idx / 7, cb = idx % 7;
  const int row0 = rb * 64, col0 = cb * 64;
  const int arow = wid * 8 + (lane >> 3), achunk = (lane & 7) ^ ((arow >> 1) & 7);
  const unsigned aoff = (unsigned)((row0 + arow) * (KT * 2) + achunk * 16);
  const unsigned boff = (unsigned)((wid * 8 + (lane >> 5) * 4 + (lane & 3)) * (D * 2) + col0 * 2 + ((lane >> 2) & 7) * 16);
  auto issue = [&](int st) {
    const unsigned dst = lds0 + (st % NSLOT) * 16384 + wid * 1024;
    glds16((const char*)a + st * 128, aoff, dst);
    glds16((const char*)w + (size_t)st * 64 * (D * 2), boff, dst + 8192);
  };
  const int rt = wid & 1, ct = (wid >> 1) & 1, h = lane >> 5;
  constexpr int EPI = NSLOT * 16384;
  const int orow = row0 + rt * 32 + (lane & 31), ocol = col0 + ct * 32 + 4 * h;
  glds16(res, (unsigned)((row0 + arow) * (D * 2) + col0 * 2 + achunk * 16), lds0 + EPI + wid * 1024);
  if (wid == 0 && lane < 16) glds16(gate, (unsigned)(col0 * 4 + lane * 16), lds0 + EPI + 8192);
#pragma unroll
  for (int st = 0; st < PRE; ++st) issue(st);
  f32x16 acc = {};
  unsigned afo[4];
#pragma unroll
  for (int s = 0; s < 4; ++s) afo[s] = rt * 4096 + a_frag_off(lane, s);
  const unsigned bfo = 8192 + b_frag_off<8>(lane, ct * 32);
  auto compute = [&](int slot) {
    if (wid < 4) {
      lds_ptr base = sm + slot * 16384;
#pragma unroll
      for (int s = 0; s < 4; ++s) {
        const bf16x8 af = *(const LAS bf16x8*)(base + afo[s]);
        const bf16x8 bf = cat8(trread(base + bfo + (4 * s) * 512), trread(base + bfo + (4 * s + 1) * 512));
        acc = MFMA32(bf, af, acc);
      }
    }
  };
  if constexpr (NST <= 8) {
#pragma unroll
    for (int st = 0; st < NST; ++st) {
      if (st == 0) waitv<(NST - 1) * 2>(); else if (st == 1) waitv<(NST - 2) * 2>(); else if (st == 2) waitv<(NST - 3) * 2>();
      else if (st == 3) waitv<(NST - 4) * 2>(); else if (st == 4) waitv<(NST - 5) * 2>(); else if (st == 5) waitv<(NST - 6) * 2>();
      else waitv<0>();
      BAR();
      compute(st);
    }
  } else {
    for (int st = 0; st < NST - PRE + 1; ++st) {
      waitv<(PRE - 1) * 2>();
      BAR();
      if (st + PRE < NST) issue(st + PRE);
      compute(st % NSLOT);
    }
#pragma unroll
    for (int j = 1; j < PRE; ++j) {
      const int st = NST - PRE + j;
      if (j == 1) waitv<(PRE - 2) * 2>(); else if (j == 2) waitv<(PRE - 3) * 2>(); else if (j == 3) waitv<(PRE - 4) * 2>();
      else if (j == 4) waitv<(PRE - 5) * 2>(); else if (j == 5) waitv<(PRE - 6) * 2>(); else waitv<0>();
      BAR();
      compute(st % NSLOT);
    }
  }
  if (wid < 4) {
    float xv[16];
    u32x2 rv[4]; f32x4 gv[4];
    {
      const int r = rt * 32 + (lane & 31);
#pragma unroll
      for (int g = 0; g < 4; ++g) {
        rv[g] = *(const LAS u32x2*)(sm + EPI + r * 128 + (((ct * 4 + g) ^ ((r >> 1) & 7)) << 4) + 8 * h);
        gv[g] = *(const LAS f32x4*)(sm + EPI + 8192 + (ct * 32 + 8 * g + 4 * h) * 4);
      }
    }
#pragma unroll
    for (int g = 0; g < 4; ++g) {
      xv[4 * g] = bfround(bflo(rv[g][0]) + bfround(gv[g][0] * bfround(acc[4 * g])));
      xv[4 * g + 1] = bfround(bfhi(rv[g][0]) + bfround(gv[g][1] * bfround(acc[4 * g + 1])));
      xv[4 * g + 2] = bfround(bflo(rv[g][1]) + bfround(gv[g][2] * bfround(acc[4 * g + 2])));
      xv[4 * g + 3] = bfround(bfhi(rv[g][1]) + bfround(gv[g][3] * bfround(acc[4 * g + 3])));
      *(u32x2*)(out + (size_t)orow * D + ocol + 8 * g) = (u32x2){cvtpk(xv[4 * g], xv[4 * g + 1]), cvtpk(xv[4 * g + 2], xv[4 * g + 3])};
    }
    if constexpr (STATS) {
      float s = 0;
#pragma unroll
      for (int r = 0; r < 16; ++r) s += xv[r];
      const float mean = swap32_add(s) * (1.f / 32.f);
      float q = 0;
#pragma unroll
      for (int r = 0; r < 16; ++r) { const float d = xv[r] - mean; q += d * d; }
      q = swap32_add(q);
      if (h == 0) st2[(size_t)orow * 16 + cb * 2 + ct] = (f32x2){mean, q};
    }
  }
}

constexpr int K4_NSLOT = 4, K4_SLOT = 32768, K4_AB = K4_NSLOT * K4_SLOT, K4_LDS = K4_AB + 4096 + 16384;
__global__ __launch_bounds__(512) void k4_ffn1(const u16* __restrict__ x1, const u16* __restrict__ w, const float* __restrict__ mod,
                                               const f32x2* __restrict__ st2, u16* __restrict__ hout) {
  extern __shared__ __attribute__((aligned(16))) char smem[];
  lds_ptr sm = (lds_ptr)smem;
  const unsigned lds0 = (unsigned)(uintptr_t)smem;
  const int tid = threadIdx.x, lane = tid & 63, wid = __builtin_amdgcn_readfirstlane(tid >> 6);
  const int xg = blockIdx.x & 7, idx = blockIdx.x >> 3, rb = xg * 2 + idx / 14, cb = idx % 14;
  const int row0 = rb * 128, col0 = cb * 128;
  const int arow = wid * 8 + (lane >> 3), achunk = (lane & 7) ^ ((arow >> 1) & 7);
  const unsigned aoff = (unsigned)((row0 + arow) * (D * 2) + achunk * 16);
  const unsigned boff = (unsigned)((wid * 4 + (lane & 3)) * (F * 2) + col0 * 2 + (lane >> 2) * 16);
  auto issue = [&](int st) {
    const unsigned dst = lds0 + (st % K4_NSLOT) * K4_SLOT + wid * 1024;
    glds16((const char*)x1 + st * 128, aoff, dst);
    glds16((const char*)x1 + st * 128 + 64 * (D * 2), aoff, dst + 8192);
    const char* wb = (const char*)w + (size_t)st * 64 * (F * 2);
    glds16(wb, boff, dst + 16384);
    glds16(wb + 32 * (F * 2), boff, dst + 16384 + 8192);
  };
  if (wid < 4) glds16(mod + 3 * D, lane * 16 + wid * 1024, lds0 + K4_AB + wid * 1024);
  glds16(st2 + (size_t)(row0 + wid * 8) * 16, lane * 16, lds0 + K4_AB + 4096 + wid * 1024);
  glds16(st2 + (size_t)(row0 + 64 + wid * 8) * 16, lane * 16, lds0 + K4_AB + 4096 + 8192 + wid * 1024);
  issue(0); issue(1); issue(2);
  waitv<12>();
  float rstd[2], nmr[2];
#pragma unroll
  for (int i = 0; i < 2; ++i) {
    f32x4 t = *(const LAS f32x4*)(sm + K4_AB + 4096 + i * 8192 + wid * 1024 + lane * 16);
    if ((lane & 7) == 7) t = (f32x4){0, 0, 0, 0};
    float s = t[0] + t[2];
    s += __shfl_xor(s, 1); s += __shfl_xor(s, 2); s += __shfl_xor(s, 4);
    const float mean = s * (1.f / 14.f);
    const float d0 = t[0] - mean, d1 = t[2] - mean;
    float q = (lane & 7) < 7 ? t[1] + t[3] + 32.f * (d0 * d0 + d1 * d1) : 0.f;
    q += __shfl_xor(q, 1); q += __shfl_xor(q, 2); q += __shfl_xor(q, 4);
    rstd[i] = 1.f / sqrtf(q * (1.f / D) + LN_EPS);
    nmr[i] = -mean * rstd[i];
  }
  BAR();
  const LAS float* bv = (const LAS float*)(sm + K4_AB) + achunk * 8;
  const LAS float* av = bv + D;
  const int wr = wid & 1, wc = (wid >> 1) & 1, h = lane >> 5;
  f32x16 acc[2][2] = {};
  unsigned afo[4], bfo[2];
#pragma unroll
  for (int s = 0; s < 4; ++s) afo[s] = wr * 8192 + a_frag_off(lane, s);
#pragma unroll
  for (int n = 0; n < 2; ++n) bfo[n] = 16384 + b_frag_off<16>(lane, wc * 64 + n * 32);
#pragma unroll
  for (int stg = 0; stg < 7; ++stg) {
    if (stg <= 4) waitv<8>(); else if (stg == 5) waitv<4>(); else waitv<0>();
    lds_ptr base = sm + (stg % K4_NSLOT) * K4_SLOT;
    ln_chunk(base + wid * 1024 + lane * 16, rstd[0], nmr[0], av + stg * 64, bv + stg * 64);
    ln_chunk(base + 8192 + wid * 1024 + lane * 16, rstd[1], nmr[1], av + stg * 64, bv + stg * 64);
    BAR();
    if (stg + 3 < 7) issue(stg + 3);
    if (wid < 4) {
#pragma unroll
      for (int s = 0; s < 4; ++s) {
        const bf16x8 a0 = *(const LAS bf16x8*)(base + afo[s]), a1 = *(const LAS bf16x8*)(base + afo[s] + 4096);
#pragma unroll
        for (int n = 0; n < 2; ++n) {
          const bf16x8 b = cat8(trread(base + bfo[n] + (4 * s) * 1024), trread(base + bfo[n] + (4 * s + 1) * 1024));
          acc[0][n] = MFMA32(b, a0, acc[0][n]);
          acc[1][n] = MFMA32(b, a1, acc[1][n]);
        }
      }
    }
  }
  if (wid < 4) {
#pragma unroll
    for (int m = 0; m < 2; ++m)
#pragma unroll
      for (int n = 0; n < 2; ++n) {
        u16* op = hout + (size_t)(row0 + wr * 64 + m * 32 + (lane & 31)) * F + col0 + wc * 64 + n * 32 + 4 * h;
#pragma unroll
        for (int g = 0; g < 4; ++g) {
          float v[4];
#pragma unroll
          for (int j = 0; j < 4; ++j) { const float z = bfround(acc[m][n][4 * g + j]); v[j] = z * __builtin_amdgcn_rcpf(1.f + __expf(-z)); }
          *(u32x2*)(op + 8 * g) = (u32x2){cvtpk(v[0], v[1]), cvtpk(v[2], v[3])};
        }
      }
  }
}

extern "C" void kernel_launch(void* const* d_in, const int* in_sizes, int n_in, void* d_out, int out_size, void* d_ws, size_t ws_size,
                              hipStream_t stream) {
  const u16* x = (const u16*)d_in[0];
  const u16* wqkv = (const u16*)d_in[1];
  const u16* wo = (const u16*)d_in[2];
  const u16* w1 = (const u16*)d_in[4];
  const u16* w2 = (const u16*)d_in[5];
  const u16* c = (const u16*)d_in[6];
  const u16* wmod = (const u16*)d_in[7];
  char* ws = (char*)d_ws;
  float* mod = (float*)(ws + WS_MOD);
  f32x2* st1 = (f32x2*)(ws + WS_ST1);
  f32x2* st2 = (f32x2*)(ws + WS_ST2);
  u16 *q = (u16*)(ws + WS_Q), *k = (u16*)(ws + WS_K), *v = (u16*)(ws + WS_V), *o = (u16*)(ws + WS_O), *x1 = (u16*)(ws + WS_X1), *hb = (u16*)(ws + WS_H);
  (void)hipFuncSetAttribute((const void*)k1_qkv, hipFuncAttributeMaxDynamicSharedMemorySize, K1_LDS);
  (void)hipFuncSetAttribute((const void*)k2_attn, hipFuncAttributeMaxDynamicSharedMemorySize, K2_LDS);
  (void)hipFuncSetAttribute((const void*)k_proj<D, true>, hipFuncAttributeMaxDynamicSharedMemorySize, 7 * 16384 + 9216);
  (void)hipFuncSetAttribute((const void*)k4_ffn1, hipFuncAttributeMaxDynamicSharedMemorySize, K4_LDS);
  (void)hipFuncSetAttribute((const void*)k_proj<F, false>, hipFuncAttributeMaxDynamicSharedMemorySize, 8 * 16384 + 9216);
  hipLaunchKernelGGL(k0_mod_stats, dim3(K0_MODB + K0_STB), dim3(256), 0, stream, x, c, wmod, mod, st1);
  hipLaunchKernelGGL(k1_qkv, dim3(256), dim3(512), K1_LDS, stream, x, wqkv, mod, st1, q, k, v);
  hipLaunchKernelGGL(k2_attn, dim3(256), dim3(512), K2_LDS, stream, q, k, v, o);
  hipLaunchKernelGGL((k_proj<D, true>), dim3(224), dim3(512), 7 * 16384 + 9216, stream, o, wo, x, mod + 2 * D, x1, st2);
  hipLaunchKernelGGL(k4_ffn1, dim3(224), dim3(512), K4_LDS, stream, x1, w1, mod, st2, hb);
  hipLaunchKernelGGL((k_proj<F, false>), dim3(224), dim3(512), 8 * 16384 + 9216, stream, hb, w2, x1, mod + 5 * D, (u16*)d_out, st2);
}
```

```cpp
#include <hip/hip_runtime.h>
#include <hip/hip_bf16.h>
#include <stdint.h>

typedef unsigned short u16;
using bf16x8 = __attribute__((ext_vector_type(8))) short;
using s16x4 = __attribute__((ext_vector_type(4))) short;
using f32x16 = __attribute__((ext_vector_type(16))) float;
using f32x4 = __attribute__((ext_vector_type(4))) float;
using f32x2 = __attribute__((ext_vector_type(2))) float;
using u32x4 = __attribute__((ext_vector_type(4))) unsigned;
using u32x2 = __attribute__((ext_vector_type(2))) unsigned;
typedef short v4i16_t __attribute__((ext_vector_type(4)));
#define LAS __attribute__((address_space(3)))
typedef LAS char* lds_ptr;
#define DI __device__ __forceinline__
#define MFMA32(a, b, c) __builtin_amdgcn_mfma_f32_32x32x16_bf16((a), (b), (c), 0, 0, 0)

constexpr int S = 2048, D = 448, NH = 16, DH = 28, F = 1792, NQKV = 3 * D, NMOD = 6 * D;
constexpr float LN_EPS = 1e-5f;
constexpr float QSCALE = 0.18898223650461362f * 1.4426950408889634f;

constexpr size_t WS_MOD = 0;
constexpr size_t WS_MODB = 11264;
constexpr size_t WS_UT = 16384;
constexpr size_t WS_ST1 = 32768;
constexpr size_t WS_ST2 = WS_ST1 + 2048 * 16;
constexpr size_t WS_Q = WS_ST2 + 2048 * 16 * 8;
constexpr size_t QKV_BYTES = (size_t)NH * 64 * 2048;
constexpr size_t WS_K = WS_Q + QKV_BYTES;
constexpr size_t WS_V = WS_K + QKV_BYTES;
constexpr size_t WS_O = WS_V + QKV_BYTES;
constexpr size_t WS_X1 = WS_O + (size_t)64 * 32 * 1024;
constexpr size_t WS_Y = WS_X1 + (size_t)64 * 28 * 1024;
constexpr size_t WS_H = WS_Y + (size_t)64 * 28 * 1024;
constexpr size_t WS_FLAGS = WS_H + (size_t)64 * 112 * 1024;
constexpr size_t WS_END = WS_FLAGS + 4 * 256 * 128;

DI float bf2f(u16 v) { return __uint_as_float((unsigned)v << 16); }
DI float bflo(unsigned w) { return __uint_as_float(w << 16); }
DI float bfhi(unsigned w) { return __uint_as_float(w & 0xffff0000u); }
DI unsigned cvtpk(float lo, float hi) {
  typedef __bf16 bf2_t __attribute__((ext_vector_type(2)));
  bf2_t r = __builtin_convertvector((f32x2){lo, hi}, bf2_t);
  return __builtin_bit_cast(unsigned, r);
}
DI float bfround(float x) { return bflo(cvtpk(x, 0.f)); }
DI void glds16(const void* sbase, unsigned voff, unsigned lds_base) {
  asm volatile("s_mov_b32 m0, %2\n\ts_nop 0\n\tglobal_load_lds_dwordx4 %0, %1" ::"v"(voff), "s"(sbase), "s"(lds_base) : "memory");
}
DI void glds16x4(const void* sbase, unsigned voff, unsigned lds_base) {
  asm volatile("s_mov_b32 m0, %2\n\ts_nop 0\n\tglobal_load_lds_dwordx4 %0, %1\n\tglobal_load_lds_dwordx4 %0, %1 offset:1024\n\t"
               "global_load_lds_dwordx4 %0, %1 offset:2048\n\tglobal_load_lds_dwordx4 %0, %1 offset:3072" ::"v"(voff), "s"(sbase), "s"(lds_base) : "memory");
}
DI void l2_touch(const void* p, int first, int n64, unsigned lds_scratch) {
  const int lane = threadIdx.x & 63;
  for (int i = first; i < n64; i += 64) {
    const int j = i + lane < n64 ? i + lane : n64 - 1;
    asm volatile("s_mov_b32 m0, %2\n\ts_nop 0\n\tglobal_load_lds_dword %0, %1" ::"v"(j * 128), "s"(p), "s"(lds_scratch) : "memory");
  }
}
template <int CTRL> DI float dpp_add(float v) {
  return v + __uint_as_float(__builtin_amdgcn_update_dpp(0u, __float_as_uint(v), CTRL, 0xf, 0xf, true));
}
DI float sum8(float v) { v = dpp_add<0xB1>(v); v = dpp_add<0x4E>(v); return dpp_add<0x141>(v); }
#define PIN(x) asm volatile("" : "+v"(x))
#define SB() __builtin_amdgcn_sched_barrier(0)
#define WAITV(N) asm volatile("s_waitcnt vmcnt(" #N ")" ::: "memory")
#define BARW() asm volatile("s_waitcnt lgkmcnt(2)\n\ts_barrier" ::: "memory")
#define BARX() asm volatile("s_barrier" ::: "memory")
#define BAR() asm volatile("s_waitcnt lgkmcnt(0)\n\ts_barrier" ::: "memory")
template <int N> DI void waitv() {
  static_assert(N >= 0 && N <= 63, "vmcnt");
  asm volatile("s_waitcnt vmcnt(%0)" ::"i"(N) : "memory");
}
DI s16x4 trread(lds_ptr p) { return __builtin_bit_cast(s16x4, __builtin_amdgcn_ds_read_tr16_b64_v4i16((LAS v4i16_t*)p)); }
DI bf16x8 cat8(s16x4 a, s16x4 b) { return __builtin_shufflevector(a, b, 0, 1, 2, 3, 4, 5, 6, 7); }
DI bf16x8 pack8(const f32x16& x, int s) {
  u32x4 w = {cvtpk(x[8 * s], x[8 * s + 1]), cvtpk(x[8 * s + 2], x[8 * s + 3]), cvtpk(x[8 * s + 4], x[8 * s + 5]), cvtpk(x[8 * s + 6], x[8 * s + 7])};
  return __builtin_bit_cast(bf16x8, w);
}
DI float swap32_max(float m) {
  auto rr = __builtin_amdgcn_permlane32_swap(__float_as_uint(m), __float_as_uint(m), false, false);
  return fmaxf(__uint_as_float(rr[0]), __uint_as_float(rr[1]));
}
DI float swap32_add(float m) {
  auto rr = __builtin_amdgcn_permlane32_swap(__float_as_uint(m), __float_as_uint(m), false, false);
  return __uint_as_float(rr[0]) + __uint_as_float(rr[1]);
}


template <int NV> DI void gemv_stripe(const u16* __restrict__ w, int ldw, int n0, const u16* __restrict__ c, float (&res)[NV], LAS float* red) {
  const int tid = threadIdx.x, lane = tid & 63, wid = tid >> 6, cg = tid & 3, rg = tid >> 2;
  u32x4 wv[7]; u16 cv[7];
  const unsigned wo_ = (unsigned)(rg * 7 * ldw + n0 + cg * 8) * 2;
#pragma unroll
  for (int i = 0; i < 7; ++i) wv[i] = *(const u32x4*)((const char*)w + (size_t)i * ldw * 2 + wo_);
#pragma unroll
  for (int i = 0; i < 7; ++i) cv[i] = c[rg * 7 + i];
  SB();
  float cf[NV][7];
#pragma unroll
  for (int i = 0; i < 7; ++i) { const float cx = bf2f(cv[i]); cf[0][i] = bfround(cx * __builtin_amdgcn_rcpf(1.f + __expf(-cx))); }
#pragma unroll
  for (int v = 0; v < NV; ++v) {
    float acc[8] = {0, 0, 0, 0, 0, 0, 0, 0};
#pragma unroll
    for (int i = 0; i < 7; ++i)
#pragma unroll
      for (int j = 0; j < 4; ++j) { acc[2 * j] += cf[v][i] * bflo(wv[i][j]); acc[2 * j + 1] += cf[v][i] * bfhi(wv[i][j]); }
#pragma unroll
    for (int j = 0; j < 8; ++j) { acc[j] = dpp_add<0x124>(acc[j]); acc[j] = dpp_add<0x128>(acc[j]); }
    if ((lane & 15) < 4) {
      LAS float* p = red + ((v * 16 + wid * 4 + (lane >> 4)) * 32 + cg * 8);
      *(LAS f32x4*)p = (f32x4){acc[0], acc[1], acc[2], acc[3]};
      *(LAS f32x4*)(p + 4) = (f32x4){acc[4], acc[5], acc[6], acc[7]};
    }
  }
  __syncthreads();
#pragma unroll
  for (int v = 0; v < NV; ++v) {
    float s = 0;
    if (tid < 32) {
#pragma unroll
      for (int i = 0; i < 16; ++i) s += red[(v * 16 + i) * 32 + tid];
    }
    res[v] = s;
  }
}

__global__ __launch_bounds__(256, 2) void k0_mod_stats(const u16* __restrict__ x, const u16* __restrict__ c, const u16* __restrict__ wmod,
                                                    const u16* __restrict__ wqkv, float* __restrict__ mod, f32x4* __restrict__ st1,
                                                    const u16* __restrict__ wo, const u16* __restrict__ w1, const u16* __restrict__ w2) {
  __shared__ __attribute__((aligned(16))) float red_[16 * 32 + 64];
  const int tid = threadIdx.x, lane = tid & 63, wid = __builtin_amdgcn_readfirstlane(tid >> 6);
  const int xg = blockIdx.x & 7, idx = blockIdx.x >> 3;
  asm volatile("" ::"s"(x), "s"(c), "s"(wmod), "s"(wqkv), "s"(mod), "s"(st1));
  if (__builtin_expect(idx < 11, 1)) {
    const int stripe = idx * 8 + xg;
    if (__builtin_expect(stripe < NMOD / 32, 1)) {
      float res[1];
      gemv_stripe<1>(wmod, NMOD, stripe * 32, c, res, (LAS float*)red_);
      if (tid < 32) {
        float v = bfround(res[0]);
        const int n = stripe * 32 + tid, which = n / D;
        if (which == 1 || which == 4) v = bfround(1.f + v);
        mod[n] = v;
        ((u16*)(mod + WS_MODB / 4))[n] = (u16)(__float_as_uint(v) >> 16);
      }
    }
  } else if (idx < 19) {
    const int row = xg * 256 + (idx - 11) * 32 + (tid >> 3);
    u32x4 w[7];
#pragma unroll
    for (int i = 0; i < 7; ++i) w[i] = *(const u32x4*)(x + (size_t)row * D + i * 64 + (tid & 7) * 8);
    float s = 0;
#pragma unroll
    for (int i = 0; i < 7; ++i)
#pragma unroll
      for (int j = 0; j < 4; ++j) s += bflo(w[i][j]) + bfhi(w[i][j]);
    const float mean = sum8(s) * (1.f / D);
    float q = 0;
#pragma unroll
    for (int i = 0; i < 7; ++i)
#pragma unroll
      for (int j = 0; j < 4; ++j) { const float a = bflo(w[i][j]) - mean, b = bfhi(w[i][j]) - mean; q += a * a + b * b; }
    const float rstd = 1.f / sqrtf(sum8(q) * (1.f / D) + LN_EPS);
    if ((tid & 7) == 0) st1[row] = (f32x4){rstd, -mean * rstd, mean, 0.f};
  } else {
    if (blockIdx.x >= 252) ((unsigned*)(mod + WS_FLAGS / 4))[((blockIdx.x - 252) * 256 + tid) * 32] = 0u;
    if (idx >= 28 && idx < 30) {
      unsigned long long pc;
      asm volatile("s_getpc_b64 s[40:41]\n\ts_add_u32 s40, s40, _Z6k1_qkvPKtS0_PKfPDv4_fPt@rel32@lo+4\n\t"
                   "s_addc_u32 s41, s41, _Z6k1_qkvPKtS0_PKfPDv4_fPt@rel32@hi+12\n\ts_mov_b64 %0, s[40:41]" : "=s"(pc) :: "scc", "s40", "s41");
      const int w = (idx - 28) * 4 + wid;
      l2_touch((const void*)pc, w * 50, w * 50 + 50, (unsigned)(uintptr_t)red_);
    }
    constexpr int NL = D * NQKV * 2 / 128;
    const int w = ((idx - 19) * 8 + xg) * 4 + wid;
    l2_touch(wqkv, w * 23, w * 23 + 23 < NL ? w * 23 + 23 : NL, (unsigned)(uintptr_t)red_);
  }
}

DI unsigned a_frag_off(int lane, int s) { const int r = lane & 31, h = lane >> 5; return r * 128 + ((((2 * s + h) ^ ((r >> 1) & 7))) << 4); }

DI u32x4 ln_chunk(u32x4 w, float rstd, float nmr, const f32x4& a0, const f32x4& a1, const f32x4& b0, const f32x4& b1) {
  float t[8];
#pragma unroll
  for (int j = 0; j < 4; ++j) { t[2 * j] = bflo(w[j]); t[2 * j + 1] = bfhi(w[j]); }
#pragma unroll
  for (int j = 0; j < 8; ++j) asm("v_fma_f32 %0, %0, %1, %2" : "+v"(t[j]) : "v"(rstd), "v"(nmr));
#pragma unroll
  for (int j = 0; j < 8; ++j) asm("v_fma_f32 %0, %0, %1, %2" : "+v"(t[j]) : "v"(j < 4 ? a0[j & 3] : a1[j & 3]), "v"(j < 4 ? b0[j & 3] : b1[j & 3]));
  return (u32x4){cvtpk(t[0], t[1]), cvtpk(t[2], t[3]), cvtpk(t[4], t[5]), cvtpk(t[6], t[7])};
}

constexpr int K1_KG = 6 * 256, K1_BSLOT = 16 * K1_KG, K1_NSLOT = 4;
constexpr int K1_A = 0, K1_B = 7 * 8192, K1_LDS = K1_B + K1_NSLOT * K1_BSLOT + 256;
template <int NV>
DI void k1_compute(lds_ptr sm, int lane, int rt, int col0, f32x16 (&acc)[3]) {
  unsigned afo[4], bfo[3];
  const int h = lane >> 5, g1 = (lane >> 4) & 1, q = (lane >> 2) & 3, p = lane & 3;
#pragma unroll
  for (int s = 0; s < 4; ++s) afo[s] = K1_A + rt * 4096 + a_frag_off(lane, s);
#pragma unroll
  for (int u = 0; u < 3; ++u) { const int col = col0 + u * DH + 16 * g1 + 4 * p; bfo[u] = K1_B + 2 * h * K1_KG + (col >> 5) * 256 + q * 64 + (col & 31) * 2; }
  unsigned bsl[3][K1_NSLOT];
#pragma unroll
  for (int u = 0; u < 3; ++u)
#pragma unroll
    for (int i = 0; i < K1_NSLOT; ++i) { bsl[u][i] = bfo[u] + i * K1_BSLOT; PIN(bsl[u][i]); }
  bf16x8 fa[4], fb[4][3];
#pragma unroll
  for (int kk = 0; kk < 30; ++kk) {
    const int stg = kk >> 2, s = kk & 3, c = (kk - 2) & 3;
    if (kk == 0) BAR(); else if (kk < 28 && s == 0) BARX();
#pragma unroll
    for (int u = 0; u < 3; ++u) {
      if (kk >= 2) {
        if (u < 3 - NV) acc[u] = MFMA32(fb[c][u], fa[c], acc[u]);
        else acc[u] = MFMA32(fa[c], fb[c][u], acc[u]);
        SB();
      }
      if (kk < 28) {
        if (u == 0) fa[s] = *(const LAS bf16x8*)(sm + afo[s] + stg * 8192);
        lds_ptr bp = sm + bsl[u][stg % K1_NSLOT] + 4 * s * K1_KG;
        fb[s][u] = cat8(trread(bp), trread(bp + K1_KG));
        SB();
      }
    }
  }
}
constexpr int K1_AB = K1_LDS, K1_LDS2 = K1_AB + 4096;
__global__ __launch_bounds__(768, 3) void k1_qkv(const u16* __restrict__ x, const u16* __restrict__ w, const float* __restrict__ mod,
                                                 f32x4* __restrict__ st1, u16* __restrict__ qkv) {
  extern __shared__ __attribute__((aligned(16))) char smem[];
  lds_ptr sm = (lds_ptr)smem;
  const unsigned lds0 = (unsigned)(uintptr_t)smem;
  const int tid = threadIdx.x, lane = tid & 63, wid = __builtin_amdgcn_readfirstlane(tid >> 6);
  const int xg = blockIdx.x & 7, idx = blockIdx.x >> 3, rb = xg * 4 + (idx >> 3), cb = idx & 7;
  const int row0 = rb * 64;
  if (wid >= 8) {
    const int lw = wid - 8;
    glds16(mod, lane * 16 + lw * 1024, lds0 + K1_AB + lw * 1024);
    const int astart = (cb * 336) & ~63;
    unsigned boff[6];
#pragma unroll
    for (int i = 0; i < 6; ++i) {
      const int blk = 4 * i + (lane >> 4), kg = 4 * lw + blk / 6, seg = blk % 6;
      boff[i] = (unsigned)((4 * kg + ((lane >> 2) & 3)) * (NQKV * 2) + astart + seg * 64 + (lane & 3) * 16);
    }
    auto issueB = [&](int st) {
      const char* wb = (const char*)w + (size_t)st * 64 * (NQKV * 2);
      const unsigned dst = lds0 + K1_B + (st % K1_NSLOT) * K1_BSLOT + lw * 6144;
#pragma unroll
      for (int i = 0; i < 6; ++i) glds16(wb, boff[i], dst + i * 1024);
    };
    issueB(0);
    waitv<6>(); BAR();
    issueB(1); issueB(2); issueB(3);
    waitv<18>(); BAR();
    waitv<12>(); BAR();
    waitv<6>(); BAR(); issueB(4);
    waitv<6>(); BAR(); issueB(5);
    waitv<6>(); BAR(); issueB(6);
    waitv<6>(); BAR();
    waitv<0>(); BAR();
  } else {
    {
      const int c8 = lane & 7, r = wid * 8 + (lane >> 3);
      const u16* xp = x + (size_t)(row0 + r) * D + c8 * 8;
      u32x4 xw[7];
#pragma unroll
      for (int st = 0; st < 7; ++st) xw[st] = *(const u32x4*)(xp + st * 64);
      const f32x2 stv = *(const f32x2*)(st1 + row0 + r);
      SB();
      BARX();
      SB();
      const f32x2 rs2 = {stv[0], stv[0]}, nm2 = {stv[1], stv[1]};
      lds_ptr dst = sm + K1_A + r * 128 + ((c8 ^ ((r >> 1) & 7)) << 4);
      const LAS f32x4* bp = (const LAS f32x4*)(sm + K1_AB) + c8 * 2;
      const LAS f32x4* ap = bp + D / 4;
      f32x4 a0 = ap[0], a1 = ap[1], b0 = bp[0], b1 = bp[1];
#pragma unroll
      for (int st = 0; st < 7; ++st) {
        f32x4 na0, na1, nb0, nb1;
        if (st < 6) { na0 = ap[(st + 1) * 16]; na1 = ap[(st + 1) * 16 + 1]; nb0 = bp[(st + 1) * 16]; nb1 = bp[(st + 1) * 16 + 1]; }
        SB();
        f32x2 t[4];
#pragma unroll
        for (int j = 0; j < 4; ++j) t[j] = (f32x2){bflo(xw[st][j]), bfhi(xw[st][j])};
#pragma unroll
        for (int j = 0; j < 4; ++j) t[j] = __builtin_elementwise_fma(t[j], rs2, nm2);
        t[0] = __builtin_elementwise_fma(t[0], (f32x2){a0[0], a0[1]}, (f32x2){b0[0], b0[1]});
        t[1] = __builtin_elementwise_fma(t[1], (f32x2){a0[2], a0[3]}, (f32x2){b0[2], b0[3]});
        t[2] = __builtin_elementwise_fma(t[2], (f32x2){a1[0], a1[1]}, (f32x2){b1[0], b1[1]});
        t[3] = __builtin_elementwise_fma(t[3], (f32x2){a1[2], a1[3]}, (f32x2){b1[2], b1[3]});
        *(LAS u32x4*)(dst + st * 8192) = (u32x4){cvtpk(t[0][0], t[0][1]), cvtpk(t[1][0], t[1][1]), cvtpk(t[2][0], t[2][1]), cvtpk(t[3][0], t[3][1])};
        SB();
        a0 = na0; a1 = na1; b0 = nb0; b1 = nb1;
      }
    }
    if (wid >= 4) { BAR(); return; }
    const int rt = wid & 1, ug = wid >> 1, unit0 = cb * 6 + ug * 3;
    const int col0 = ((cb * 336) & 63) / 2 + ug * 3 * DH;
    f32x16 acc[3] = {};
    if (unit0 < 30) k1_compute<0>(sm, lane, rt, col0, acc);
    else if (unit0 == 30) k1_compute<1>(sm, lane, rt, col0, acc);
    else k1_compute<3>(sm, lane, rt, col0, acc);
    const int h = lane >> 5, tile = rb * 2 + rt;
#pragma unroll
    for (int u = 0; u < 3; ++u) {
      const int unit = unit0 + u, typ = unit >> 4;
      f32x16 v = acc[u];
      if (typ == 0) {
#pragma unroll
        for (int r = 0; r < 16; ++r) v[r] *= QSCALE;
        if (h) { v[12] = 0; v[13] = 0; v[14] = 0; v[15] = 0; }
      } else if (typ == 1) {
        if (h) { v[12] = 1.f; v[13] = 0; v[14] = 0; v[15] = 0; }
      } else {
        const int d = lane & 31;
        if (d >= DH) {
          const float pv = d == DH ? 1.f : 0.f;
#pragma unroll
          for (int r = 0; r < 16; ++r) v[r] = pv;
        }
      }
      u16* p = qkv + ((size_t)(unit * 64 + tile) * 2) * 512 + lane * 8;
      asm volatile("global_store_dwordx4 %0, %1, off sc1\n\ts_nop 1" ::"v"(p), "v"(pack8(v, 0)) : "memory");
      asm volatile("global_store_dwordx4 %0, %1, off offset:1024 sc1\n\ts_nop 1" ::"v"(p), "v"(pack8(v, 1)) : "memory");
    }
  }
}

#define ATTN_ASM \
  "global_load_dwordx4 v[128:131], %[voff], %[kb0] offset:0\n\t" \
  "global_load_dwordx4 v[132:135], %[voff], %[kb0] offset:1024\n\t" \
  "global_load_dwordx4 v[64:67], %[voff], %[qb0] offset:0\n\t" \
  "global_load_dwordx4 v[68:71], %[voff], %[qb0] offset:1024\n\t" \
  "global_load_dwordx4 v[72:75], %[voff], %[qb1] offset:0\n\t" \
  "global_load_dwordx4 v[76:79], %[voff], %[qb1] offset:1024\n\t" \
  "global_load_dwordx4 v[136:139], %[voff], %[vb0] offset:0\n\t" \
  "global_load_dwordx4 v[140:143], %[voff], %[vb0] offset:1024\n\t" \
  "global_load_dwordx4 v[144:147], %[voff], %[kb0] offset:2048\n\t" \
  "global_load_dwordx4 v[148:151], %[voff], %[kb0] offset:3072\n\t" \
  "global_load_dwordx4 v[152:155], %[voff], %[vb0] offset:2048\n\t" \
  "global_load_dwordx4 v[156:159], %[voff], %[vb0] offset:3072\n\t" \
  "global_load_dwordx4 v[160:163], %[voff], %[kb1] offset:0\n\t" \
  "global_load_dwordx4 v[164:167], %[voff], %[kb1] offset:1024\n\t" \
  "s_mov_b32 s40, 0\n\t" \
  "s_mov_b32 s41, -1\n\t" \
  "s_waitcnt vmcnt(8)\n\t" \
  "v_mfma_f32_32x32x16_bf16 v[80:95], v[128:131], v[64:67], 0\n\t" \
  "v_mfma_f32_32x32x16_bf16 v[96:111], v[128:131], v[72:75], 0\n\t" \
  "v_mfma_f32_32x32x16_bf16 v[80:95], v[132:135], v[68:71], v[80:95]\n\t" \
  "v_mfma_f32_32x32x16_bf16 v[96:111], v[132:135], v[76:79], v[96:111]\n\t" \
  "s_nop 11\n\t" \
  "v_max3_f32 v112, v80, v81, v82\n\t" \
  "v_max3_f32 v112, v112, v83, v84\n\t" \
  "v_max3_f32 v112, v112, v85, v86\n\t" \
  "v_max3_f32 v112, v112, v87, v88\n\t" \
  "v_max3_f32 v112, v112, v89, v90\n\t" \
  "v_max3_f32 v112, v112, v91, v92\n\t" \
  "v_max3_f32 v112, v112, v93, v94\n\t" \
  "v_max_f32 v112, v112, v95\n\t" \
  "v_mov_b32 v113, v112\n\t" \
  "s_nop 1\n\t" \
  "v_permlane32_swap_b32 v112, v113\n\t" \
  "s_nop 0\n\t" \
  "v_max_f32 v112, v112, v113\n\t" \
  "v_cvt_pk_bf16_f32 %[nm0], -v112, 0\n\t" \
  "v_cndmask_b32 v70, v70, %[nm0], s[40:41]\n\t" \
  "s_nop 1\n\t" \
  "v_max3_f32 v120, v96, v97, v98\n\t" \
  "v_max3_f32 v120, v120, v99, v100\n\t" \
  "v_max3_f32 v120, v120, v101, v102\n\t" \
  "v_max3_f32 v120, v120, v103, v104\n\t" \
  "v_max3_f32 v120, v120, v105, v106\n\t" \
  "v_max3_f32 v120, v120, v107, v108\n\t" \
  "v_max3_f32 v120, v120, v109, v110\n\t" \
  "v_max_f32 v120, v120, v111\n\t" \
  "v_mov_b32 v121, v120\n\t" \
  "s_nop 1\n\t" \
  "v_permlane32_swap_b32 v120, v121\n\t" \
  "s_nop 0\n\t" \
  "v_max_f32 v120, v120, v121\n\t" \
  "v_cvt_pk_bf16_f32 %[nm1], -v120, 0\n\t" \
  "v_cndmask_b32 v78, v78, %[nm1], s[40:41]\n\t" \
  "s_nop 1\n\t" \
  "v_mfma_f32_32x32x16_bf16 v[80:95], v[128:131], v[64:67], 0\n\t" \
  "v_mfma_f32_32x32x16_bf16 v[80:95], v[132:135], v[68:71], v[80:95]\n\t" \
  "s_nop 11\n\t" \
  "v_mfma_f32_32x32x16_bf16 v[96:111], v[128:131], v[72:75], 0\n\t" \
  "v_exp_f32 v80, v80\n\t" \
  "v_exp_f32 v81, v81\n\t" \
  "v_exp_f32 v82, v82\n\t" \
  "v_exp_f32 v83, v83\n\t" \
  "v_exp_f32 v84, v84\n\t" \
  "v_exp_f32 v85, v85\n\t" \
  "v_exp_f32 v86, v86\n\t" \
  "v_exp_f32 v87, v87\n\t" \
  "v_cvt_pk_bf16_f32 v112, v80, v81\n\t" \
  "v_cvt_pk_bf16_f32 v113, v82, v83\n\t" \
  "v_mfma_f32_32x32x16_bf16 v[96:111], v[132:135], v[76:79], v[96:111]\n\t" \
  "v_exp_f32 v88, v88\n\t" \
  "v_exp_f32 v89, v89\n\t" \
  "v_exp_f32 v90, v90\n\t" \
  "v_exp_f32 v91, v91\n\t" \
  "v_cvt_pk_bf16_f32 v114, v84, v85\n\t" \
  "v_cvt_pk_bf16_f32 v115, v86, v87\n\t" \
  "v_exp_f32 v92, v92\n\t" \
  "v_exp_f32 v93, v93\n\t" \
  "v_exp_f32 v94, v94\n\t" \
  "v_exp_f32 v95, v95\n\t" \
  "v_cvt_pk_bf16_f32 v116, v88, v89\n\t" \
  "v_cvt_pk_bf16_f32 v117, v90, v91\n\t" \
  "global_load_dwordx4 v[176:179], %[voff], %[kb1] offset:2048\n\t" \
  "global_load_dwordx4 v[180:183], %[voff], %[kb1] offset:3072\n\t" \
  "global_load_dwordx4 v[168:171], %[voff], %[vb1] offset:0\n\t" \
  "global_load_dwordx4 v[172:175], %[voff], %[vb1] offset:1024\n\t" \
  "global_load_dwordx4 v[192:195], %[voff], %[kb2] offset:0\n\t" \
  "global_load_dwordx4 v[196:199], %[voff], %[kb2] offset:1024\n\t" \
  "global_load_dwordx4 v[184:187], %[voff], %[vb1] offset:2048\n\t" \
  "global_load_dwordx4 v[188:191], %[voff], %[vb1] offset:3072\n\t" \
  "s_waitcnt vmcnt(12)\n\t" \
  "v_cvt_pk_bf16_f32 v118, v92, v93\n\t" \
  "v_cvt_pk_bf16_f32 v119, v94, v95\n\t" \
  "v_mfma_f32_32x32x16_bf16 v[80:95], v[144:147], v[64:67], 0\n\t" \
  "v_exp_f32 v96, v96\n\t" \
  "v_exp_f32 v97, v97\n\t" \
  "v_exp_f32 v98, v98\n\t" \
  "v_exp_f32 v99, v99\n\t" \
  "v_mfma_f32_32x32x16_bf16 %[o0], v[136:139], v[112:115], %[o0]\n\t" \
  "v_exp_f32 v100, v100\n\t" \
  "v_exp_f32 v101, v101\n\t" \
  "v_exp_f32 v102, v102\n\t" \
  "v_exp_f32 v103, v103\n\t" \
  "v_cvt_pk_bf16_f32 v120, v96, v97\n\t" \
  "v_cvt_pk_bf16_f32 v121, v98, v99\n\t" \
  "v_mfma_f32_32x32x16_bf16 v[80:95], v[148:151], v[68:71], v[80:95]\n\t" \
  "v_exp_f32 v104, v104\n\t" \
  "v_exp_f32 v105, v105\n\t" \
  "v_exp_f32 v106, v106\n\t" \
  "v_exp_f32 v107, v107\n\t" \
  "v_cvt_pk_bf16_f32 v122, v100, v101\n\t" \
  "v_cvt_pk_bf16_f32 v123, v102, v103\n\t" \
  "v_mfma_f32_32x32x16_bf16 %[o0], v[140:143], v[116:119], %[o0]\n\t" \
  "v_exp_f32 v108, v108\n\t" \
  "v_exp_f32 v109, v109\n\t" \
  "v_exp_f32 v110, v110\n\t" \
  "v_exp_f32 v111, v111\n\t" \
  "v_cvt_pk_bf16_f32 v124, v104, v105\n\t" \
  "v_cvt_pk_bf16_f32 v125, v106, v107\n\t" \
  "v_cvt_pk_bf16_f32 v126, v108, v109\n\t" \
  "v_cvt_pk_bf16_f32 v127, v110, v111\n\t" \
  "v_mfma_f32_32x32x16_bf16 v[96:111], v[144:147], v[72:75], 0\n\t" \
  "v_exp_f32 v80, v80\n\t" \
  "v_exp_f32 v81, v81\n\t" \
  "v_exp_f32 v82, v82\n\t" \
  "v_exp_f32 v83, v83\n\t" \
  "v_mfma_f32_32x32x16_bf16 %[o1], v[136:139], v[120:123], %[o1]\n\t" \
  "v_exp_f32 v84, v84\n\t" \
  "v_exp_f32 v85, v85\n\t" \
  "v_exp_f32 v86, v86\n\t" \
  "v_exp_f32 v87, v87\n\t" \
  "v_cvt_pk_bf16_f32 v112, v80, v81\n\t" \
  "v_cvt_pk_bf16_f32 v113, v82, v83\n\t" \
  "v_mfma_f32_32x32x16_bf16 v[96:111], v[148:151], v[76:79], v[96:111]\n\t" \
  "v_exp_f32 v88, v88\n\t" \
  "v_exp_f32 v89, v89\n\t" \
  "v_exp_f32 v90, v90\n\t" \
  "v_exp_f32 v91, v91\n\t" \
  "v_cvt_pk_bf16_f32 v114, v84, v85\n\t" \
  "v_cvt_pk_bf16_f32 v115, v86, v87\n\t" \
  "v_mfma_f32_32x32x16_bf16 %[o1], v[140:143], v[124:127], %[o1]\n\t" \
  "v_exp_f32 v92, v92\n\t" \
  "v_exp_f32 v93, v93\n\t" \
  "v_exp_f32 v94, v94\n\t" \
  "v_exp_f32 v95, v95\n\t" \
  "v_cvt_pk_bf16_f32 v116, v88, v89\n\t" \
  "v_cvt_pk_bf16_f32 v117, v90, v91\n\t" \
  "global_load_dwordx4 v[208:211], %[voff], %[kb2] offset:2048\n\t" \
  "global_load_dwordx4 v[212:215], %[voff], %[kb2] offset:3072\n\t" \
  "global_load_dwordx4 v[200:203], %[voff], %[vb2] offset:0\n\t" \
  "global_load_dwordx4 v[204:207], %[voff], %[vb2] offset:1024\n\t" \
  "global_load_dwordx4 v[224:227], %[voff], %[kb3] offset:0\n\t" \
  "global_load_dwordx4 v[228:231], %[voff], %[kb3] offset:1024\n\t" \
  "global_load_dwordx4 v[216:219], %[voff], %[vb2] offset:2048\n\t" \
  "global_load_dwordx4 v[220:223], %[voff], %[vb2] offset:3072\n\t" \
  "s_waitcnt vmcnt(16)\n\t" \
  "v_cvt_pk_bf16_f32 v118, v92, v93\n\t" \
  "v_cvt_pk_bf16_f32 v119, v94, v95\n\t" \
  "v_mfma_f32_32x32x16_bf16 v[80:95], v[160:163], v[64:67], 0\n\t" \
  "v_exp_f32 v96, v96\n\t" \
  "v_exp_f32 v97, v97\n\t" \
  "v_exp_f32 v98, v98\n\t" \
  "v_exp_f32 v99, v99\n\t" \
  "v_mfma_f32_32x32x16_bf16 %[o0], v[152:155], v[112:115], %[o0]\n\t" \
  "v_exp_f32 v100, v100\n\t" \
  "v_exp_f32 v101, v101\n\t" \
  "v_exp_f32 v102, v102\n\t" \
  "v_exp_f32 v103, v103\n\t" \
  "v_cvt_pk_bf16_f32 v120, v96, v97\n\t" \
  "v_cvt_pk_bf16_f32 v121, v98, v99\n\t" \
  "v_mfma_f32_32x32x16_bf16 v[80:95], v[164:167], v[68:71], v[80:95]\n\t" \
  "v_exp_f32 v104, v104\n\t" \
  "v_exp_f32 v105, v105\n\t" \
  "v_exp_f32 v106, v106\n\t" \
  "v_exp_f32 v107, v107\n\t" \
  "v_cvt_pk_bf16_f32 v122, v100, v101\n\t" \
  "v_cvt_pk_bf16_f32 v123, v102, v103\n\t" \
  "v_mfma_f32_32x32x16_bf16 %[o0], v[156:159], v[116:119], %[o0]\n\t" \
  "v_exp_f32 v108, v108\n\t" \
  "v_exp_f32 v109, v109\n\t" \
  "v_exp_f32 v110, v110\n\t" \
  "v_exp_f32 v111, v111\n\t" \
  "v_cvt_pk_bf16_f32 v124, v104, v105\n\t" \
  "v_cvt_pk_bf16_f32 v125, v106, v107\n\t" \
  "v_cvt_pk_bf16_f32 v126, v108, v109\n\t" \
  "v_cvt_pk_bf16_f32 v127, v110, v111\n\t" \
  "v_mfma_f32_32x32x16_bf16 v[96:111], v[160:163], v[72:75], 0\n\t" \
  "v_exp_f32 v80, v80\n\t" \
  "v_exp_f32 v81, v81\n\t" \
  "v_exp_f32 v82, v82\n\t" \
  "v_exp_f32 v83, v83\n\t" \
  "v_mfma_f32_32x32x16_bf16 %[o1], v[152:155], v[120:123], %[o1]\n\t" \
  "v_exp_f32 v84, v84\n\t" \
  "v_exp_f32 v85, v85\n\t" \
  "v_exp_f32 v86, v86\n\t" \
  "v_exp_f32 v87, v87\n\t" \
  "v_cvt_pk_bf16_f32 v112, v80, v81\n\t" \
  "v_cvt_pk_bf16_f32 v113, v82, v83\n\t" \
  "v_mfma_f32_32x32x16_bf16 v[96:111], v[164:167], v[76:79], v[96:111]\n\t" \
  "v_exp_f32 v88, v88\n\t" \
  "v_exp_f32 v89, v89\n\t" \
  "v_exp_f32 v90, v90\n\t" \
  "v_exp_f32 v91, v91\n\t" \
  "v_cvt_pk_bf16_f32 v114, v84, v85\n\t" \
  "v_cvt_pk_bf16_f32 v115, v86, v87\n\t" \
  "v_mfma_f32_32x32x16_bf16 %[o1], v[156:159], v[124:127], %[o1]\n\t" \
  "v_exp_f32 v92, v92\n\t" \
  "v_exp_f32 v93, v93\n\t" \
  "v_exp_f32 v94, v94\n\t" \
  "v_exp_f32 v95, v95\n\t" \
  "v_cvt_pk_bf16_f32 v116, v88, v89\n\t" \
  "v_cvt_pk_bf16_f32 v117, v90, v91\n\t" \
  "global_load_dwordx4 v[240:243], %[voff], %[kb3] offset:2048\n\t" \
  "global_load_dwordx4 v[244:247], %[voff], %[kb3] offset:3072\n\t" \
  "global_load_dwordx4 v[232:235], %[voff], %[vb3] offset:0\n\t" \
  "global_load_dwordx4 v[236:239], %[voff], %[vb3] offset:1024\n\t" \
  "v_add_u32 %[voff], 0x2000, %[voff]\n\t" \
  "global_load_dwordx4 v[128:131], %[voff], %[kb2] offset:0\n\t" \
  "global_load_dwordx4 v[132:135], %[voff], %[kb2] offset:1024\n\t" \
  "global_load_dwordx4 v[248:251], %[voff], %[vb1] offset:2048\n\t" \
  "global_load_dwordx4 v[252:255], %[voff], %[vb1] offset:3072\n\t" \
  "s_waitcnt vmcnt(20)\n\t" \
  "v_cvt_pk_bf16_f32 v118, v92, v93\n\t" \
  "v_cvt_pk_bf16_f32 v119, v94, v95\n\t" \
  "v_mfma_f32_32x32x16_bf16 v[80:95], v[176:179], v[64:67], 0\n\t" \
  "v_exp_f32 v96, v96\n\t" \
  "v_exp_f32 v97, v97\n\t" \
  "v_exp_f32 v98, v98\n\t" \
  "v_exp_f32 v99, v99\n\t" \
  "v_mfma_f32_32x32x16_bf16 %[o0], v[168:171], v[112:115], %[o0]\n\t" \
  "v_exp_f32 v100, v100\n\t" \
  "v_exp_f32 v101, v101\n\t" \
  "v_exp_f32 v102, v102\n\t" \
  "v_exp_f32 v103, v103\n\t" \
  "v_cvt_pk_bf16_f32 v120, v96, v97\n\t" \
  "v_cvt_pk_bf16_f32 v121, v98, v99\n\t" \
  "v_mfma_f32_32x32x16_bf16 v[80:95], v[180:183], v[68:71], v[80:95]\n\t" \
  "v_exp_f32 v104, v104\n\t" \
  "v_exp_f32 v105, v105\n\t" \
  "v_exp_f32 v106, v106\n\t" \
  "v_exp_f32 v107, v107\n\t" \
  "v_cvt_pk_bf16_f32 v122, v100, v101\n\t" \
  "v_cvt_pk_bf16_f32 v123, v102, v103\n\t" \
  "v_mfma_f32_32x32x16_bf16 %[o0], v[172:175], v[116:119], %[o0]\n\t" \
  "v_exp_f32 v108, v108\n\t" \
  "v_exp_f32 v109, v109\n\t" \
  "v_exp_f32 v110, v110\n\t" \
  "v_exp_f32 v111, v111\n\t" \
  "v_cvt_pk_bf16_f32 v124, v104, v105\n\t" \
  "v_cvt_pk_bf16_f32 v125, v106, v107\n\t" \
  "v_cvt_pk_bf16_f32 v126, v108, v109\n\t" \
  "v_cvt_pk_bf16_f32 v127, v110, v111\n\t" \
  "v_mfma_f32_32x32x16_bf16 v[96:111], v[176:179], v[72:75], 0\n\t" \
  "v_exp_f32 v80, v80\n\t" \
  "v_exp_f32 v81, v81\n\t" \
  "v_exp_f32 v82, v82\n\t" \
  "v_exp_f32 v83, v83\n\t" \
  "v_mfma_f32_32x32x16_bf16 %[o1], v[168:171], v[120:123], %[o1]\n\t" \
  "v_exp_f32 v84, v84\n\t" \
  "v_exp_f32 v85, v85\n\t" \
  "v_exp_f32 v86, v86\n\t" \
  "v_exp_f32 v87, v87\n\t" \
  "v_cvt_pk_bf16_f32 v112, v80, v81\n\t" \
  "v_cvt_pk_bf16_f32 v113, v82, v83\n\t" \
  "v_mfma_f32_32x32x16_bf16 v[96:111], v[180:183], v[76:79], v[96:111]\n\t" \
  "v_exp_f32 v88, v88\n\t" \
  "v_exp_f32 v89, v89\n\t" \
  "v_exp_f32 v90, v90\n\t" \
  "v_exp_f32 v91, v91\n\t" \
  "v_cvt_pk_bf16_f32 v114, v84, v85\n\t" \
  "v_cvt_pk_bf16_f32 v115, v86, v87\n\t" \
  "v_mfma_f32_32x32x16_bf16 %[o1], v[172:175], v[124:127], %[o1]\n\t" \
  "v_exp_f32 v92, v92\n\t" \
  "v_exp_f32 v93, v93\n\t" \
  "v_exp_f32 v94, v94\n\t" \
  "v_exp_f32 v95, v95\n\t" \
  "v_cvt_pk_bf16_f32 v116, v88, v89\n\t" \
  "v_cvt_pk_bf16_f32 v117, v90, v91\n\t" \
  "global_load_dwordx4 v[144:147], %[voff], %[kb2] offset:2048\n\t" \
  "global_load_dwordx4 v[148:151], %[voff], %[kb2] offset:3072\n\t" \
  "global_load_dwordx4 v[136:139], %[voff], %[vb2] offset:0\n\t" \
  "global_load_dwordx4 v[140:143], %[voff], %[vb2] offset:1024\n\t" \
  "global_load_dwordx4 v[160:163], %[voff], %[kb3] offset:0\n\t" \
  "global_load_dwordx4 v[164:167], %[voff], %[kb3] offset:1024\n\t" \
  "global_load_dwordx4 v[152:155], %[voff], %[vb2] offset:2048\n\t" \
  "global_load_dwordx4 v[156:159], %[voff], %[vb2] offset:3072\n\t" \
  "s_waitcnt vmcnt(24)\n\t" \
  "v_cvt_pk_bf16_f32 v118, v92, v93\n\t" \
  "v_cvt_pk_bf16_f32 v119, v94, v95\n\t" \
  "v_mfma_f32_32x32x16_bf16 v[80:95], v[192:195], v[64:67], 0\n\t" \
  "v_exp_f32 v96, v96\n\t" \
  "v_exp_f32 v97, v97\n\t" \
  "v_exp_f32 v98, v98\n\t" \
  "v_exp_f32 v99, v99\n\t" \
  "v_mfma_f32_32x32x16_bf16 %[o0], v[184:187], v[112:115], %[o0]\n\t" \
  "v_exp_f32 v100, v100\n\t" \
  "v_exp_f32 v101, v101\n\t" \
  "v_exp_f32 v102, v102\n\t" \
  "v_exp_f32 v103, v103\n\t" \
  "v_cvt_pk_bf16_f32 v120, v96, v97\n\t" \
  "v_cvt_pk_bf16_f32 v121, v98, v99\n\t" \
  "v_mfma_f32_32x32x16_bf16 v[80:95], v[196:199], v[68:71], v[80:95]\n\t" \
  "v_exp_f32 v104, v104\n\t" \
  "v_exp_f32 v105, v105\n\t" \
  "v_exp_f32 v106, v106\n\t" \
  "v_exp_f32 v107, v107\n\t" \
  "v_cvt_pk_bf16_f32 v122, v100, v101\n\t" \
  "v_cvt_pk_bf16_f32 v123, v102, v103\n\t" \
  "v_mfma_f32_32x32x16_bf16 %[o0], v[188:191], v[116:119], %[o0]\n\t" \
  "v_exp_f32 v108, v108\n\t" \
  "v_exp_f32 v109, v109\n\t" \
  "v_exp_f32 v110, v110\n\t" \
  "v_exp_f32 v111, v111\n\t" \
  "v_cvt_pk_bf16_f32 v124, v104, v105\n\t" \
  "v_cvt_pk_bf16_f32 v125, v106, v107\n\t" \
  "v_cvt_pk_bf16_f32 v126, v108, v109\n\t" \
  "v_cvt_pk_bf16_f32 v127, v110, v111\n\t" \
  "v_mfma_f32_32x32x16_bf16 v[96:111], v[192:195], v[72:75], 0\n\t" \
  "v_exp_f32 v80, v80\n\t" \
  "v_exp_f32 v81, v81\n\t" \
  "v_exp_f32 v82, v82\n\t" \
  "v_exp_f32 v83, v83\n\t" \
  "v_mfma_f32_32x32x16_bf16 %[o1], v[184:187], v[120:123], %[o1]\n\t" \
  "v_exp_f32 v84, v84\n\t" \
  "v_exp_f32 v85, v85\n\t" \
  "v_exp_f32 v86, v86\n\t" \
  "v_exp_f32 v87, v87\n\t" \
  "v_cvt_pk_bf16_f32 v112, v80, v81\n\t" \
  "v_cvt_pk_bf16_f32 v113, v82, v83\n\t" \
  "v_mfma_f32_32x32x16_bf16 v[96:111], v[196:199], v[76:79], v[96:111]\n\t" \
  "v_exp_f32 v88, v88\n\t" \
  "v_exp_f32 v89, v89\n\t" \
  "v_exp_f32 v90, v90\n\t" \
  "v_exp_f32 v91, v91\n\t" \
  "v_cvt_pk_bf16_f32 v114, v84, v85\n\t" \
  "v_cvt_pk_bf16_f32 v115, v86, v87\n\t" \
  "v_mfma_f32_32x32x16_bf16 %[o1], v[188:191], v[124:127], %[o1]\n\t" \
  "v_exp_f32 v92, v92\n\t" \
  "v_exp_f32 v93, v93\n\t" \
  "v_exp_f32 v94, v94\n\t" \
  "v_exp_f32 v95, v95\n\t" \
  "v_cvt_pk_bf16_f32 v116, v88, v89\n\t" \
  "v_cvt_pk_bf16_f32 v117, v90, v91\n\t" \
  "global_load_dwordx4 v[176:179], %[voff], %[kb3] offset:2048\n\t" \
  "global_load_dwordx4 v[180:183], %[voff], %[kb3] offset:3072\n\t" \
  "global_load_dwordx4 v[168:171], %[voff], %[vb3] offset:0\n\t" \
  "global_load_dwordx4 v[172:175], %[voff], %[vb3] offset:1024\n\t" \
  "v_add_u32 %[voff], 0x2000, %[voff]\n\t" \
  "global_load_dwordx4 v[192:195], %[voff], %[kb2] offset:0\n\t" \
  "global_load_dwordx4 v[196:199], %[voff], %[kb2] offset:1024\n\t" \
  "global_load_dwordx4 v[184:187], %[voff], %[vb1] offset:2048\n\t" \
  "global_load_dwordx4 v[188:191], %[voff], %[vb1] offset:3072\n\t" \
  "s_waitcnt vmcnt(28)\n\t" \
  "v_cvt_pk_bf16_f32 v118, v92, v93\n\t" \
  "v_cvt_pk_bf16_f32 v119, v94, v95\n\t" \
  "v_mfma_f32_32x32x16_bf16 v[80:95], v[208:211], v[64:67], 0\n\t" \
  "v_exp_f32 v96, v96\n\t" \
  "v_exp_f32 v97, v97\n\t" \
  "v_exp_f32 v98, v98\n\t" \
  "v_exp_f32 v99, v99\n\t" \
  "v_mfma_f32_32x32x16_bf16 %[o0], v[200:203], v[112:115], %[o0]\n\t" \
  "v_exp_f32 v100, v100\n\t" \
  "v_exp_f32 v101, v101\n\t" \
  "v_exp_f32 v102, v102\n\t" \
  "v_exp_f32 v103, v103\n\t" \
  "v_cvt_pk_bf16_f32 v120, v96, v97\n\t" \
  "v_cvt_pk_bf16_f32 v121, v98, v99\n\t" \
  "v_mfma_f32_32x32x16_bf16 v[80:95], v[212:215], v[68:71], v[80:95]\n\t" \
  "v_exp_f32 v104, v104\n\t" \
  "v_exp_f32 v105, v105\n\t" \
  "v_exp_f32 v106, v106\n\t" \
  "v_exp_f32 v107, v107\n\t" \
  "v_cvt_pk_bf16_f32 v122, v100, v101\n\t" \
  "v_cvt_pk_bf16_f32 v123, v102, v103\n\t" \
  "v_mfma_f32_32x32x16_bf16 %[o0], v[204:207], v[116:119], %[o0]\n\t" \
  "v_exp_f32 v108, v108\n\t" \
  "v_exp_f32 v109, v109\n\t" \
  "v_exp_f32 v110, v110\n\t" \
  "v_exp_f32 v111, v111\n\t" \
  "v_cvt_pk_bf16_f32 v124, v104, v105\n\t" \
  "v_cvt_pk_bf16_f32 v125, v106, v107\n\t" \
  "v_cvt_pk_bf16_f32 v126, v108, v109\n\t" \
  "v_cvt_pk_bf16_f32 v127, v110, v111\n\t" \
  "v_mfma_f32_32x32x16_bf16 v[96:111], v[208:211], v[72:75], 0\n\t" \
  "v_exp_f32 v80, v80\n\t" \
  "v_exp_f32 v81, v81\n\t" \
  "v_exp_f32 v82, v82\n\t" \
  "v_exp_f32 v83, v83\n\t" \
  "v_mfma_f32_32x32x16_bf16 %[o1], v[200:203], v[120:123], %[o1]\n\t" \
  "v_exp_f32 v84, v84\n\t" \
  "v_exp_f32 v85, v85\n\t" \
  "v_exp_f32 v86, v86\n\t" \
  "v_exp_f32 v87, v87\n\t" \
  "v_cvt_pk_bf16_f32 v112, v80, v81\n\t" \
  "v_cvt_pk_bf16_f32 v113, v82, v83\n\t" \
  "v_mfma_f32_32x32x16_bf16 v[96:111], v[212:215], v[76:79], v[96:111]\n\t" \
  "v_exp_f32 v88, v88\n\t" \
  "v_exp_f32 v89, v89\n\t" \
  "v_exp_f32 v90, v90\n\t" \
  "v_exp_f32 v91, v91\n\t" \
  "v_cvt_pk_bf16_f32 v114, v84, v85\n\t" \
  "v_cvt_pk_bf16_f32 v115, v86, v87\n\t" \
  "v_mfma_f32_32x32x16_bf16 %[o1], v[204:207], v[124:127], %[o1]\n\t" \
  "v_exp_f32 v92, v92\n\t" \
  "v_exp_f32 v93, v93\n\t" \
  "v_exp_f32 v94, v94\n\t" \
  "v_exp_f32 v95, v95\n\t" \
  "v_cvt_pk_bf16_f32 v116, v88, v89\n\t" \
  "v_cvt_pk_bf16_f32 v117, v90, v91\n\t" \
  "global_load_dwordx4 v[208:211], %[voff], %[kb2] offset:2048\n\t" \
  "global_load_dwordx4 v[212:215], %[voff], %[kb2] offset:3072\n\t" \
  "global_load_dwordx4 v[200:203], %[voff], %[vb2] offset:0\n\t" \
  "global_load_dwordx4 v[204:207], %[voff], %[vb2] offset:1024\n\t" \
  "s_waitcnt vmcnt(28)\n\t" \
  "v_cvt_pk_bf16_f32 v118, v92, v93\n\t" \
  "v_cvt_pk_bf16_f32 v119, v94, v95\n\t" \
  "v_mfma_f32_32x32x16_bf16 v[80:95], v[224:227], v[64:67], 0\n\t" \
  "v_exp_f32 v96, v96\n\t" \
  "v_exp_f32 v97, v97\n\t" \
  "v_exp_f32 v98, v98\n\t" \
  "v_exp_f32 v99, v99\n\t" \
  "v_mfma_f32_32x32x16_bf16 %[o0], v[216:219], v[112:115], %[o0]\n\t" \
  "v_exp_f32 v100, v100\n\t" \
  "v_exp_f32 v101, v101\n\t" \
  "v_exp_f32 v102, v102\n\t" \
  "v_exp_f32 v103, v103\n\t" \
  "v_cvt_pk_bf16_f32 v120, v96, v97\n\t" \
  "v_cvt_pk_bf16_f32 v121, v98, v99\n\t" \
  "v_mfma_f32_32x32x16_bf16 v[80:95], v[228:231], v[68:71], v[80:95]\n\t" \
  "v_exp_f32 v104, v104\n\t" \
  "v_exp_f32 v105, v105\n\t" \
  "v_exp_f32 v106, v106\n\t" \
  "v_exp_f32 v107, v107\n\t" \
  "v_cvt_pk_bf16_f32 v122, v100, v101\n\t" \
  "v_cvt_pk_bf16_f32 v123, v102, v103\n\t" \
  "v_mfma_f32_32x32x16_bf16 %[o0], v[220:223], v[116:119], %[o0]\n\t" \
  "v_exp_f32 v108, v108\n\t" \
  "v_exp_f32 v109, v109\n\t" \
  "v_exp_f32 v110, v110\n\t" \
  "v_exp_f32 v111, v111\n\t" \
  "v_cvt_pk_bf16_f32 v124, v104, v105\n\t" \
  "v_cvt_pk_bf16_f32 v125, v106, v107\n\t" \
  "v_cvt_pk_bf16_f32 v126, v108, v109\n\t" \
  "v_cvt_pk_bf16_f32 v127, v110, v111\n\t" \
  "v_mfma_f32_32x32x16_bf16 v[96:111], v[224:227], v[72:75], 0\n\t" \
  "v_exp_f32 v80, v80\n\t" \
  "v_exp_f32 v81, v81\n\t" \
  "v_exp_f32 v82, v82\n\t" \
  "v_exp_f32 v83, v83\n\t" \
  "v_mfma_f32_32x32x16_bf16 %[o1], v[216:219], v[120:123], %[o1]\n\t" \
  "v_exp_f32 v84, v84\n\t" \
  "v_exp_f32 v85, v85\n\t" \
  "v_exp_f32 v86, v86\n\t" \
  "v_exp_f32 v87, v87\n\t" \
  "v_cvt_pk_bf16_f32 v112, v80, v81\n\t" \
  "v_cvt_pk_bf16_f32 v113, v82, v83\n\t" \
  "v_mfma_f32_32x32x16_bf16 v[96:111], v[228:231], v[76:79], v[96:111]\n\t" \
  "v_exp_f32 v88, v88\n\t" \
  "v_exp_f32 v89, v89\n\t" \
  "v_exp_f32 v90, v90\n\t" \
  "v_exp_f32 v91, v91\n\t" \
  "v_cvt_pk_bf16_f32 v114, v84, v85\n\t" \
  "v_cvt_pk_bf16_f32 v115, v86, v87\n\t" \
  "v_mfma_f32_32x32x16_bf16 %[o1], v[220:223], v[124:127], %[o1]\n\t" \
  "v_exp_f32 v92, v92\n\t" \
  "v_exp_f32 v93, v93\n\t" \
  "v_exp_f32 v94, v94\n\t" \
  "v_exp_f32 v95, v95\n\t" \
  "v_cvt_pk_bf16_f32 v116, v88, v89\n\t" \
  "v_cvt_pk_bf16_f32 v117, v90, v91\n\t" \
  "global_load_dwordx4 v[224:227], %[voff], %[kb3] offset:0\n\t" \
  "global_load_dwordx4 v[228:231], %[voff], %[kb3] offset:1024\n\t" \
  "global_load_dwordx4 v[216:219], %[voff], %[vb2] offset:2048\n\t" \
  "global_load_dwordx4 v[220:223], %[voff], %[vb2] offset:3072\n\t" \
  "s_waitcnt vmcnt(28)\n\t" \
  "v_cvt_pk_bf16_f32 v118, v92, v93\n\t" \
  "v_cvt_pk_bf16_f32 v119, v94, v95\n\t" \
  "v_mfma_f32_32x32x16_bf16 v[80:95], v[240:243], v[64:67], 0\n\t" \
  "v_exp_f32 v96, v96\n\t" \
  "v_exp_f32 v97, v97\n\t" \
  "v_exp_f32 v98, v98\n\t" \
  "v_exp_f32 v99, v99\n\t" \
  "v_mfma_f32_32x32x16_bf16 %[o0], v[232:235], v[112:115], %[o0]\n\t" \
  "v_exp_f32 v100, v100\n\t" \
  "v_exp_f32 v101, v101\n\t" \
  "v_exp_f32 v102, v102\n\t" \
  "v_exp_f32 v103, v103\n\t" \
  "v_cvt_pk_bf16_f32 v120, v96, v97\n\t" \
  "v_cvt_pk_bf16_f32 v121, v98, v99\n\t" \
  "v_mfma_f32_32x32x16_bf16 v[80:95], v[244:247], v[68:71], v[80:95]\n\t" \
  "v_exp_f32 v104, v104\n\t" \
  "v_exp_f32 v105, v105\n\t" \
  "v_exp_f32 v106, v106\n\t" \
  "v_exp_f32 v107, v107\n\t" \
  "v_cvt_pk_bf16_f32 v122, v100, v101\n\t" \
  "v_cvt_pk_bf16_f32 v123, v102, v103\n\t" \
  "v_mfma_f32_32x32x16_bf16 %[o0], v[236:239], v[116:119], %[o0]\n\t" \
  "v_exp_f32 v108, v108\n\t" \
  "v_exp_f32 v109, v109\n\t" \
  "v_exp_f32 v110, v110\n\t" \
  "v_exp_f32 v111, v111\n\t" \
  "v_cvt_pk_bf16_f32 v124, v104, v105\n\t" \
  "v_cvt_pk_bf16_f32 v125, v106, v107\n\t" \
  "v_cvt_pk_bf16_f32 v126, v108, v109\n\t" \
  "v_cvt_pk_bf16_f32 v127, v110, v111\n\t" \
  "v_mfma_f32_32x32x16_bf16 v[96:111], v[240:243], v[72:75], 0\n\t" \
  "v_exp_f32 v80, v80\n\t" \
  "v_exp_f32 v81, v81\n\t" \
  "v_exp_f32 v82, v82\n\t" \
  "v_exp_f32 v83, v83\n\t" \
  "v_mfma_f32_32x32x16_bf16 %[o1], v[232:235], v[120:123], %[o1]\n\t" \
  "v_exp_f32 v84, v84\n\t" \
  "v_exp_f32 v85, v85\n\t" \
  "v_exp_f32 v86, v86\n\t" \
  "v_exp_f32 v87, v87\n\t" \
  "v_cvt_pk_bf16_f32 v112, v80, v81\n\t" \
  "v_cvt_pk_bf16_f32 v113, v82, v83\n\t" \
  "v_mfma_f32_32x32x16_bf16 v[96:111], v[244:247], v[76:79], v[96:111]\n\t" \
  "v_exp_f32 v88, v88\n\t" \
  "v_exp_f32 v89, v89\n\t" \
  "v_exp_f32 v90, v90\n\t" \
  "v_exp_f32 v91, v91\n\t" \
  "v_cvt_pk_bf16_f32 v114, v84, v85\n\t" \
  "v_cvt_pk_bf16_f32 v115, v86, v87\n\t" \
  "v_mfma_f32_32x32x16_bf16 %[o1], v[236:239], v[124:127], %[o1]\n\t" \
  "v_exp_f32 v92, v92\n\t" \
  "v_exp_f32 v93, v93\n\t" \
  "v_exp_f32 v94, v94\n\t" \
  "v_exp_f32 v95, v95\n\t" \
  "v_cvt_pk_bf16_f32 v116, v88, v89\n\t" \
  "v_cvt_pk_bf16_f32 v117, v90, v91\n\t" \
  "global_load_dwordx4 v[240:243], %[voff], %[kb3] offset:2048\n\t" \
  "global_load_dwordx4 v[244:247], %[voff], %[kb3] offset:3072\n\t" \
  "global_load_dwordx4 v[232:235], %[voff], %[vb3] offset:0\n\t" \
  "global_load_dwordx4 v[236:239], %[voff], %[vb3] offset:1024\n\t" \
  "s_waitcnt vmcnt(28)\n\t" \
  "v_cvt_pk_bf16_f32 v118, v92, v93\n\t" \
  "v_cvt_pk_bf16_f32 v119, v94, v95\n\t" \
  "v_mfma_f32_32x32x16_bf16 v[80:95], v[128:131], v[64:67], 0\n\t" \
  "v_exp_f32 v96, v96\n\t" \
  "v_exp_f32 v97, v97\n\t" \
  "v_exp_f32 v98, v98\n\t" \
  "v_exp_f32 v99, v99\n\t" \
  "v_mfma_f32_32x32x16_bf16 %[o0], v[248:251], v[112:115], %[o0]\n\t" \
  "v_exp_f32 v100, v100\n\t" \
  "v_exp_f32 v101, v101\n\t" \
  "v_exp_f32 v102, v102\n\t" \
  "v_exp_f32 v103, v103\n\t" \
  "v_cvt_pk_bf16_f32 v120, v96, v97\n\t" \
  "v_cvt_pk_bf16_f32 v121, v98, v99\n\t" \
  "v_mfma_f32_32x32x16_bf16 v[80:95], v[132:135], v[68:71], v[80:95]\n\t" \
  "v_exp_f32 v104, v104\n\t" \
  "v_exp_f32 v105, v105\n\t" \
  "v_exp_f32 v106, v106\n\t" \
  "v_exp_f32 v107, v107\n\t" \
  "v_cvt_pk_bf16_f32 v122, v100, v101\n\t" \
  "v_cvt_pk_bf16_f32 v123, v102, v103\n\t" \
  "v_mfma_f32_32x32x16_bf16 %[o0], v[252:255], v[116:119], %[o0]\n\t" \
  "v_exp_f32 v108, v108\n\t" \
  "v_exp_f32 v109, v109\n\t" \
  "v_exp_f32 v110, v110\n\t" \
  "v_exp_f32 v111, v111\n\t" \
  "v_cvt_pk_bf16_f32 v124, v104, v105\n\t" \
  "v_cvt_pk_bf16_f32 v125, v106, v107\n\t" \
  "s_mov_b32 s42, 2\n\t" \
  "1:\n\t" \
  "v_cvt_pk_bf16_f32 v126, v108, v109\n\t" \
  "v_cvt_pk_bf16_f32 v127, v110, v111\n\t" \
  "v_mfma_f32_32x32x16_bf16 v[96:111], v[128:131], v[72:75], 0\n\t" \
  "v_exp_f32 v80, v80\n\t" \
  "v_exp_f32 v81, v81\n\t" \
  "v_exp_f32 v82, v82\n\t" \
  "v_exp_f32 v83, v83\n\t" \
  "v_mfma_f32_32x32x16_bf16 %[o1], v[248:251], v[120:123], %[o1]\n\t" \
  "v_exp_f32 v84, v84\n\t" \
  "v_exp_f32 v85, v85\n\t" \
  "v_exp_f32 v86, v86\n\t" \
  "v_exp_f32 v87, v87\n\t" \
  "v_cvt_pk_bf16_f32 v112, v80, v81\n\t" \
  "v_cvt_pk_bf16_f32 v113, v82, v83\n\t" \
  "v_mfma_f32_32x32x16_bf16 v[96:111], v[132:135], v[76:79], v[96:111]\n\t" \
  "v_exp_f32 v88, v88\n\t" \
  "v_exp_f32 v89, v89\n\t" \
  "v_exp_f32 v90, v90\n\t" \
  "v_exp_f32 v91, v91\n\t" \
  "v_cvt_pk_bf16_f32 v114, v84, v85\n\t" \
  "v_cvt_pk_bf16_f32 v115, v86, v87\n\t" \
  "v_mfma_f32_32x32x16_bf16 %[o1], v[252:255], v[124:127], %[o1]\n\t" \
  "v_exp_f32 v92, v92\n\t" \
  "v_exp_f32 v93, v93\n\t" \
  "v_exp_f32 v94, v94\n\t" \
  "v_exp_f32 v95, v95\n\t" \
  "v_cvt_pk_bf16_f32 v116, v88, v89\n\t" \
  "v_cvt_pk_bf16_f32 v117, v90, v91\n\t" \
  "v_add_u32 %[voff], 0x2000, %[voff]\n\t" \
  "global_load_dwordx4 v[128:131], %[voff], %[kb2] offset:0\n\t" \
  "global_load_dwordx4 v[132:135], %[voff], %[kb2] offset:1024\n\t" \
  "global_load_dwordx4 v[248:251], %[voff], %[vb1] offset:2048\n\t" \
  "global_load_dwordx4 v[252:255], %[voff], %[vb1] offset:3072\n\t" \
  "s_waitcnt vmcnt(28)\n\t" \
  "v_cvt_pk_bf16_f32 v118, v92, v93\n\t" \
  "v_cvt_pk_bf16_f32 v119, v94, v95\n\t" \
  "v_mfma_f32_32x32x16_bf16 v[80:95], v[144:147], v[64:67], 0\n\t" \
  "v_exp_f32 v96, v96\n\t" \
  "v_exp_f32 v97, v97\n\t" \
  "v_exp_f32 v98, v98\n\t" \
  "v_exp_f32 v99, v99\n\t" \
  "v_mfma_f32_32x32x16_bf16 %[o0], v[136:139], v[112:115], %[o0]\n\t" \
  "v_exp_f32 v100, v100\n\t" \
  "v_exp_f32 v101, v101\n\t" \
  "v_exp_f32 v102, v102\n\t" \
  "v_exp_f32 v103, v103\n\t" \
  "v_cvt_pk_bf16_f32 v120, v96, v97\n\t" \
  "v_cvt_pk_bf16_f32 v121, v98, v99\n\t" \
  "v_mfma_f32_32x32x16_bf16 v[80:95], v[148:151], v[68:71], v[80:95]\n\t" \
  "v_exp_f32 v104, v104\n\t" \
  "v_exp_f32 v105, v105\n\t" \
  "v_exp_f32 v106, v106\n\t" \
  "v_exp_f32 v107, v107\n\t" \
  "v_cvt_pk_bf16_f32 v122, v100, v101\n\t" \
  "v_cvt_pk_bf16_f32 v123, v102, v103\n\t" \
  "v_mfma_f32_32x32x16_bf16 %[o0], v[140:143], v[116:119], %[o0]\n\t" \
  "v_exp_f32 v108, v108\n\t" \
  "v_exp_f32 v109, v109\n\t" \
  "v_exp_f32 v110, v110\n\t" \
  "v_exp_f32 v111, v111\n\t" \
  "v_cvt_pk_bf16_f32 v124, v104, v105\n\t" \
  "v_cvt_pk_bf16_f32 v125, v106, v107\n\t" \
  "v_cvt_pk_bf16_f32 v126, v108, v109\n\t" \
  "v_cvt_pk_bf16_f32 v127, v110, v111\n\t" \
  "v_mfma_f32_32x32x16_bf16 v[96:111], v[144:147], v[72:75], 0\n\t" \
  "v_exp_f32 v80, v80\n\t" \
  "v_exp_f32 v81, v81\n\t" \
  "v_exp_f32 v82, v82\n\t" \
  "v_exp_f32 v83, v83\n\t" \
  "v_mfma_f32_32x32x16_bf16 %[o1], v[136:139], v[120:123], %[o1]\n\t" \
  "v_exp_f32 v84, v84\n\t" \
  "v_exp_f32 v85, v85\n\t" \
  "v_exp_f32 v86, v86\n\t" \
  "v_exp_f32 v87, v87\n\t" \
  "v_cvt_pk_bf16_f32 v112, v80, v81\n\t" \
  "v_cvt_pk_bf16_f32 v113, v82, v83\n\t" \
  "v_mfma_f32_32x32x16_bf16 v[96:111], v[148:151], v[76:79], v[96:111]\n\t" \
  "v_exp_f32 v88, v88\n\t" \
  "v_exp_f32 v89, v89\n\t" \
  "v_exp_f32 v90, v90\n\t" \
  "v_exp_f32 v91, v91\n\t" \
  "v_cvt_pk_bf16_f32 v114, v84, v85\n\t" \
  "v_cvt_pk_bf16_f32 v115, v86, v87\n\t" \
  "v_mfma_f32_32x32x16_bf16 %[o1], v[140:143], v[124:127], %[o1]\n\t" \
  "v_exp_f32 v92, v92\n\t" \
  "v_exp_f32 v93, v93\n\t" \
  "v_exp_f32 v94, v94\n\t" \
  "v_exp_f32 v95, v95\n\t" \
  "v_cvt_pk_bf16_f32 v116, v88, v89\n\t" \
  "v_cvt_pk_bf16_f32 v117, v90, v91\n\t" \
  "global_load_dwordx4 v[144:147], %[voff], %[kb2] offset:2048\n\t" \
  "global_load_dwordx4 v[148:151], %[voff], %[kb2] offset:3072\n\t" \
  "global_load_dwordx4 v[136:139], %[voff], %[vb2] offset:0\n\t" \
  "global_load_dwordx4 v[140:143], %[voff], %[vb2] offset:1024\n\t" \
  "s_waitcnt vmcnt(28)\n\t" \
  "v_cvt_pk_bf16_f32 v118, v92, v93\n\t" \
  "v_cvt_pk_bf16_f32 v119, v94, v95\n\t" \
  "v_mfma_f32_32x32x16_bf16 v[80:95], v[160:163], v[64:67], 0\n\t" \
  "v_exp_f32 v96, v96\n\t" \
  "v_exp_f32 v97, v97\n\t" \
  "v_exp_f32 v98, v98\n\t" \
  "v_exp_f32 v99, v99\n\t" \
  "v_mfma_f32_32x32x16_bf16 %[o0], v[152:155], v[112:115], %[o0]\n\t" \
  "v_exp_f32 v100, v100\n\t" \
  "v_exp_f32 v101, v101\n\t" \
  "v_exp_f32 v102, v102\n\t" \
  "v_exp_f32 v103, v103\n\t" \
  "v_cvt_pk_bf16_f32 v120, v96, v97\n\t" \
  "v_cvt_pk_bf16_f32 v121, v98, v99\n\t" \
  "v_mfma_f32_32x32x16_bf16 v[80:95], v[164:167], v[68:71], v[80:95]\n\t" \
  "v_exp_f32 v104, v104\n\t" \
  "v_exp_f32 v105, v105\n\t" \
  "v_exp_f32 v106, v106\n\t" \
  "v_exp_f32 v107, v107\n\t" \
  "v_cvt_pk_bf16_f32 v122, v100, v101\n\t" \
  "v_cvt_pk_bf16_f32 v123, v102, v103\n\t" \
  "v_mfma_f32_32x32x16_bf16 %[o0], v[156:159], v[116:119], %[o0]\n\t" \
  "v_exp_f32 v108, v108\n\t" \
  "v_exp_f32 v109, v109\n\t" \
  "v_exp_f32 v110, v110\n\t" \
  "v_exp_f32 v111, v111\n\t" \
  "v_cvt_pk_bf16_f32 v124, v104, v105\n\t" \
  "v_cvt_pk_bf16_f32 v125, v106, v107\n\t" \
  "v_cvt_pk_bf16_f32 v126, v108, v109\n\t" \
  "v_cvt_pk_bf16_f32 v127, v110, v111\n\t" \
  "v_mfma_f32_32x32x16_bf16 v[96:111], v[160:163], v[72:75], 0\n\t" \
  "v_exp_f32 v80, v80\n\t" \
  "v_exp_f32 v81, v81\n\t" \
  "v_exp_f32 v82, v82\n\t" \
  "v_exp_f32 v83, v83\n\t" \
  "v_mfma_f32_32x32x16_bf16 %[o1], v[152:155], v[120:123], %[o1]\n\t" \
  "v_exp_f32 v84, v84\n\t" \
  "v_exp_f32 v85, v85\n\t" \
  "v_exp_f32 v86, v86\n\t" \
  "v_exp_f32 v87, v87\n\t" \
  "v_cvt_pk_bf16_f32 v112, v80, v81\n\t" \
  "v_cvt_pk_bf16_f32 v113, v82, v83\n\t" \
  "v_mfma_f32_32x32x16_bf16 v[96:111], v[164:167], v[76:79], v[96:111]\n\t" \
  "v_exp_f32 v88, v88\n\t" \
  "v_exp_f32 v89, v89\n\t" \
  "v_exp_f32 v90, v90\n\t" \
  "v_exp_f32 v91, v91\n\t" \
  "v_cvt_pk_bf16_f32 v114, v84, v85\n\t" \
  "v_cvt_pk_bf16_f32 v115, v86, v87\n\t" \
  "v_mfma_f32_32x32x16_bf16 %[o1], v[156:159], v[124:127], %[o1]\n\t" \
  "v_exp_f32 v92, v92\n\t" \
  "v_exp_f32 v93, v93\n\t" \
  "v_exp_f32 v94, v94\n\t" \
  "v_exp_f32 v95, v95\n\t" \
  "v_cvt_pk_bf16_f32 v116, v88, v89\n\t" \
  "v_cvt_pk_bf16_f32 v117, v90, v91\n\t" \
  "global_load_dwordx4 v[160:163], %[voff], %[kb3] offset:0\n\t" \
  "global_load_dwordx4 v[164:167], %[voff], %[kb3] offset:1024\n\t" \
  "global_load_dwordx4 v[152:155], %[voff], %[vb2] offset:2048\n\t" \
  "global_load_dwordx4 v[156:159], %[voff], %[vb2] offset:3072\n\t" \
  "s_waitcnt vmcnt(28)\n\t" \
  "v_cvt_pk_bf16_f32 v118, v92, v93\n\t" \
  "v_cvt_pk_bf16_f32 v119, v94, v95\n\t" \
  "v_mfma_f32_32x32x16_bf16 v[80:95], v[176:179], v[64:67], 0\n\t" \
  "v_exp_f32 v96, v96\n\t" \
  "v_exp_f32 v97, v97\n\t" \
  "v_exp_f32 v98, v98\n\t" \
  "v_exp_f32 v99, v99\n\t" \
  "v_mfma_f32_32x32x16_bf16 %[o0], v[168:171], v[112:115], %[o0]\n\t" \
  "v_exp_f32 v100, v100\n\t" \
  "v_exp_f32 v101, v101\n\t" \
  "v_exp_f32 v102, v102\n\t" \
  "v_exp_f32 v103, v103\n\t" \
  "v_cvt_pk_bf16_f32 v120, v96, v97\n\t" \
  "v_cvt_pk_bf16_f32 v121, v98, v99\n\t" \
  "v_mfma_f32_32x32x16_bf16 v[80:95], v[180:183], v[68:71], v[80:95]\n\t" \
  "v_exp_f32 v104, v104\n\t" \
  "v_exp_f32 v105, v105\n\t" \
  "v_exp_f32 v106, v106\n\t" \
  "v_exp_f32 v107, v107\n\t" \
  "v_cvt_pk_bf16_f32 v122, v100, v101\n\t" \
  "v_cvt_pk_bf16_f32 v123, v102, v103\n\t" \
  "v_mfma_f32_32x32x16_bf16 %[o0], v[172:175], v[116:119], %[o0]\n\t" \
  "v_exp_f32 v108, v108\n\t" \
  "v_exp_f32 v109, v109\n\t" \
  "v_exp_f32 v110, v110\n\t" \
  "v_exp_f32 v111, v111\n\t" \
  "v_cvt_pk_bf16_f32 v124, v104, v105\n\t" \
  "v_cvt_pk_bf16_f32 v125, v106, v107\n\t" \
  "v_cvt_pk_bf16_f32 v126, v108, v109\n\t" \
  "v_cvt_pk_bf16_f32 v127, v110, v111\n\t" \
  "v_mfma_f32_32x32x16_bf16 v[96:111], v[176:179], v[72:75], 0\n\t" \
  "v_exp_f32 v80, v80\n\t" \
  "v_exp_f32 v81, v81\n\t" \
  "v_exp_f32 v82, v82\n\t" \
  "v_exp_f32 v83, v83\n\t" \
  "v_mfma_f32_32x32x16_bf16 %[o1], v[168:171], v[120:123], %[o1]\n\t" \
  "v_exp_f32 v84, v84\n\t" \
  "v_exp_f32 v85, v85\n\t" \
  "v_exp_f32 v86, v86\n\t" \
  "v_exp_f32 v87, v87\n\t" \
  "v_cvt_pk_bf16_f32 v112, v80, v81\n\t" \
  "v_cvt_pk_bf16_f32 v113, v82, v83\n\t" \
  "v_mfma_f32_32x32x16_bf16 v[96:111], v[180:183], v[76:79], v[96:111]\n\t" \
  "v_exp_f32 v88, v88\n\t" \
  "v_exp_f32 v89, v89\n\t" \
  "v_exp_f32 v90, v90\n\t" \
  "v_exp_f32 v91, v91\n\t" \
  "v_cvt_pk_bf16_f32 v114, v84, v85\n\t" \
  "v_cvt_pk_bf16_f32 v115, v86, v87\n\t" \
  "v_mfma_f32_32x32x16_bf16 %[o1], v[172:175], v[124:127], %[o1]\n\t" \
  "v_exp_f32 v92, v92\n\t" \
  "v_exp_f32 v93, v93\n\t" \
  "v_exp_f32 v94, v94\n\t" \
  "v_exp_f32 v95, v95\n\t" \
  "v_cvt_pk_bf16_f32 v116, v88, v89\n\t" \
  "v_cvt_pk_bf16_f32 v117, v90, v91\n\t" \
  "global_load_dwordx4 v[176:179], %[voff], %[kb3] offset:2048\n\t" \
  "global_load_dwordx4 v[180:183], %[voff], %[kb3] offset:3072\n\t" \
  "global_load_dwordx4 v[168:171], %[voff], %[vb3] offset:0\n\t" \
  "global_load_dwordx4 v[172:175], %[voff], %[vb3] offset:1024\n\t" \
  "s_waitcnt vmcnt(28)\n\t" \
  "v_cvt_pk_bf16_f32 v118, v92, v93\n\t" \
  "v_cvt_pk_bf16_f32 v119, v94, v95\n\t" \
  "v_mfma_f32_32x32x16_bf16 v[80:95], v[192:195], v[64:67], 0\n\t" \
  "v_exp_f32 v96, v96\n\t" \
  "v_exp_f32 v97, v97\n\t" \
  "v_exp_f32 v98, v98\n\t" \
  "v_exp_f32 v99, v99\n\t" \
  "v_mfma_f32_32x32x16_bf16 %[o0], v[184:187], v[112:115], %[o0]\n\t" \
  "v_exp_f32 v100, v100\n\t" \
  "v_exp_f32 v101, v101\n\t" \
  "v_exp_f32 v102, v102\n\t" \
  "v_exp_f32 v103, v103\n\t" \
  "v_cvt_pk_bf16_f32 v120, v96, v97\n\t" \
  "v_cvt_pk_bf16_f32 v121, v98, v99\n\t" \
  "v_mfma_f32_32x32x16_bf16 v[80:95], v[196:199], v[68:71], v[80:95]\n\t" \
  "v_exp_f32 v104, v104\n\t" \
  "v_exp_f32 v105, v105\n\t" \
  "v_exp_f32 v106, v106\n\t" \
  "v_exp_f32 v107, v107\n\t" \
  "v_cvt_pk_bf16_f32 v122, v100, v101\n\t" \
  "v_cvt_pk_bf16_f32 v123, v102, v103\n\t" \
  "v_mfma_f32_32x32x16_bf16 %[o0], v[188:191], v[116:119], %[o0]\n\t" \
  "v_exp_f32 v108, v108\n\t" \
  "v_exp_f32 v109, v109\n\t" \
  "v_exp_f32 v110, v110\n\t" \
  "v_exp_f32 v111, v111\n\t" \
  "v_cvt_pk_bf16_f32 v124, v104, v105\n\t" \
  "v_cvt_pk_bf16_f32 v125, v106, v107\n\t" \
  "v_cvt_pk_bf16_f32 v126, v108, v109\n\t" \
  "v_cvt_pk_bf16_f32 v127, v110, v111\n\t" \
  "v_mfma_f32_32x32x16_bf16 v[96:111], v[192:195], v[72:75], 0\n\t" \
  "v_exp_f32 v80, v80\n\t" \
  "v_exp_f32 v81, v81\n\t" \
  "v_exp_f32 v82, v82\n\t" \
  "v_exp_f32 v83, v83\n\t" \
  "v_mfma_f32_32x32x16_bf16 %[o1], v[184:187], v[120:123], %[o1]\n\t" \
  "v_exp_f32 v84, v84\n\t" \
  "v_exp_f32 v85, v85\n\t" \
  "v_exp_f32 v86, v86\n\t" \
  "v_exp_f32 v87, v87\n\t" \
  "v_cvt_pk_bf16_f32 v112, v80, v81\n\t" \
  "v_cvt_pk_bf16_f32 v113, v82, v83\n\t" \
  "v_mfma_f32_32x32x16_bf16 v[96:111], v[196:199], v[76:79], v[96:111]\n\t" \
  "v_exp_f32 v88, v88\n\t" \
  "v_exp_f32 v89, v89\n\t" \
  "v_exp_f32 v90, v90\n\t" \
  "v_exp_f32 v91, v91\n\t" \
  "v_cvt_pk_bf16_f32 v114, v84, v85\n\t" \
  "v_cvt_pk_bf16_f32 v115, v86, v87\n\t" \
  "v_mfma_f32_32x32x16_bf16 %[o1], v[188:191], v[124:127], %[o1]\n\t" \
  "v_exp_f32 v92, v92\n\t" \
  "v_exp_f32 v93, v93\n\t" \
  "v_exp_f32 v94, v94\n\t" \
  "v_exp_f32 v95, v95\n\t" \
  "v_cvt_pk_bf16_f32 v116, v88, v89\n\t" \
  "v_cvt_pk_bf16_f32 v117, v90, v91\n\t" \
  "v_add_u32 %[voff], 0x2000, %[voff]\n\t" \
  "global_load_dwordx4 v[192:195], %[voff], %[kb2] offset:0\n\t" \
  "global_load_dwordx4 v[196:199], %[voff], %[kb2] offset:1024\n\t" \
  "global_load_dwordx4 v[184:187], %[voff], %[vb1] offset:2048\n\t" \
  "global_load_dwordx4 v[188:191], %[voff], %[vb1] offset:3072\n\t" \
  "s_waitcnt vmcnt(28)\n\t" \
  "v_cvt_pk_bf16_f32 v118, v92, v93\n\t" \
  "v_cvt_pk_bf16_f32 v119, v94, v95\n\t" \
  "v_mfma_f32_32x32x16_bf16 v[80:95], v[208:211], v[64:67], 0\n\t" \
  "v_exp_f32 v96, v96\n\t" \
  "v_exp_f32 v97, v97\n\t" \
  "v_exp_f32 v98, v98\n\t" \
  "v_exp_f32 v99, v99\n\t" \
  "v_mfma_f32_32x32x16_bf16 %[o0], v[200:203], v[112:115], %[o0]\n\t" \
  "v_exp_f32 v100, v100\n\t" \
  "v_exp_f32 v101, v101\n\t" \
  "v_exp_f32 v102, v102\n\t" \
  "v_exp_f32 v103, v103\n\t" \
  "v_cvt_pk_bf16_f32 v120, v96, v97\n\t" \
  "v_cvt_pk_bf16_f32 v121, v98, v99\n\t" \
  "v_mfma_f32_32x32x16_bf16 v[80:95], v[212:215], v[68:71], v[80:95]\n\t" \
  "v_exp_f32 v104, v104\n\t" \
  "v_exp_f32 v105, v105\n\t" \
  "v_exp_f32 v106, v106\n\t" \
  "v_exp_f32 v107, v107\n\t" \
  "v_cvt_pk_bf16_f32 v122, v100, v101\n\t" \
  "v_cvt_pk_bf16_f32 v123, v102, v103\n\t" \
  "v_mfma_f32_32x32x16_bf16 %[o0], v[204:207], v[116:119], %[o0]\n\t" \
  "v_exp_f32 v108, v108\n\t" \
  "v_exp_f32 v109, v109\n\t" \
  "v_exp_f32 v110, v110\n\t" \
  "v_exp_f32 v111, v111\n\t" \
  "v_cvt_pk_bf16_f32 v124, v104, v105\n\t" \
  "v_cvt_pk_bf16_f32 v125, v106, v107\n\t" \
  "v_cvt_pk_bf16_f32 v126, v108, v109\n\t" \
  "v_cvt_pk_bf16_f32 v127, v110, v111\n\t" \
  "v_mfma_f32_32x32x16_bf16 v[96:111], v[208:211], v[72:75], 0\n\t" \
  "v_exp_f32 v80, v80\n\t" \
  "v_exp_f32 v81, v81\n\t" \
  "v_exp_f32 v82, v82\n\t" \
  "v_exp_f32 v83, v83\n\t" \
  "v_mfma_f32_32x32x16_bf16 %[o1], v[200:203], v[120:123], %[o1]\n\t" \
  "v_exp_f32 v84, v84\n\t" \
  "v_exp_f32 v85, v85\n\t" \
  "v_exp_f32 v86, v86\n\t" \
  "v_exp_f32 v87, v87\n\t" \
  "v_cvt_pk_bf16_f32 v112, v80, v81\n\t" \
  "v_cvt_pk_bf16_f32 v113, v82, v83\n\t" \
  "v_mfma_f32_32x32x16_bf16 v[96:111], v[212:215], v[76:79], v[96:111]\n\t" \
  "v_exp_f32 v88, v88\n\t" \
  "v_exp_f32 v89, v89\n\t" \
  "v_exp_f32 v90, v90\n\t" \
  "v_exp_f32 v91, v91\n\t" \
  "v_cvt_pk_bf16_f32 v114, v84, v85\n\t" \
  "v_cvt_pk_bf16_f32 v115, v86, v87\n\t" \
  "v_mfma_f32_32x32x16_bf16 %[o1], v[204:207], v[124:127], %[o1]\n\t" \
  "v_exp_f32 v92, v92\n\t" \
  "v_exp_f32 v93, v93\n\t" \
  "v_exp_f32 v94, v94\n\t" \
  "v_exp_f32 v95, v95\n\t" \
  "v_cvt_pk_bf16_f32 v116, v88, v89\n\t" \
  "v_cvt_pk_bf16_f32 v117, v90, v91\n\t" \
  "global_load_dwordx4 v[208:211], %[voff], %[kb2] offset:2048\n\t" \
  "global_load_dwordx4 v[212:215], %[voff], %[kb2] offset:3072\n\t" \
  "global_load_dwordx4 v[200:203], %[voff], %[vb2] offset:0\n\t" \
  "global_load_dwordx4 v[204:207], %[voff], %[vb2] offset:1024\n\t" \
  "s_waitcnt vmcnt(28)\n\t" \
  "v_cvt_pk_bf16_f32 v118, v92, v93\n\t" \
  "v_cvt_pk_bf16_f32 v119, v94, v95\n\t" \
  "v_mfma_f32_32x32x16_bf16 v[80:95], v[224:227], v[64:67], 0\n\t" \
  "v_exp_f32 v96, v96\n\t" \
  "v_exp_f32 v97, v97\n\t" \
  "v_exp_f32 v98, v98\n\t" \
  "v_exp_f32 v99, v99\n\t" \
  "v_mfma_f32_32x32x16_bf16 %[o0], v[216:219], v[112:115], %[o0]\n\t" \
  "v_exp_f32 v100, v100\n\t" \
  "v_exp_f32 v101, v101\n\t" \
  "v_exp_f32 v102, v102\n\t" \
  "v_exp_f32 v103, v103\n\t" \
  "v_cvt_pk_bf16_f32 v120, v96, v97\n\t" \
  "v_cvt_pk_bf16_f32 v121, v98, v99\n\t" \
  "v_mfma_f32_32x32x16_bf16 v[80:95], v[228:231], v[68:71], v[80:95]\n\t" \
  "v_exp_f32 v104, v104\n\t" \
  "v_exp_f32 v105, v105\n\t" \
  "v_exp_f32 v106, v106\n\t" \
  "v_exp_f32 v107, v107\n\t" \
  "v_cvt_pk_bf16_f32 v122, v100, v101\n\t" \
  "v_cvt_pk_bf16_f32 v123, v102, v103\n\t" \
  "v_mfma_f32_32x32x16_bf16 %[o0], v[220:223], v[116:119], %[o0]\n\t" \
  "v_exp_f32 v108, v108\n\t" \
  "v_exp_f32 v109, v109\n\t" \
  "v_exp_f32 v110, v110\n\t" \
  "v_exp_f32 v111, v111\n\t" \
  "v_cvt_pk_bf16_f32 v124, v104, v105\n\t" \
  "v_cvt_pk_bf16_f32 v125, v106, v107\n\t" \
  "v_cvt_pk_bf16_f32 v126, v108, v109\n\t" \
  "v_cvt_pk_bf16_f32 v127, v110, v111\n\t" \
  "v_mfma_f32_32x32x16_bf16 v[96:111], v[224:227], v[72:75], 0\n\t" \
  "v_exp_f32 v80, v80\n\t" \
  "v_exp_f32 v81, v81\n\t" \
  "v_exp_f32 v82, v82\n\t" \
  "v_exp_f32 v83, v83\n\t" \
  "v_mfma_f32_32x32x16_bf16 %[o1], v[216:219], v[120:123], %[o1]\n\t" \
  "v_exp_f32 v84, v84\n\t" \
  "v_exp_f32 v85, v85\n\t" \
  "v_exp_f32 v86, v86\n\t" \
  "v_exp_f32 v87, v87\n\t" \
  "v_cvt_pk_bf16_f32 v112, v80, v81\n\t" \
  "v_cvt_pk_bf16_f32 v113, v82, v83\n\t" \
  "v_mfma_f32_32x32x16_bf16 v[96:111], v[228:231], v[76:79], v[96:111]\n\t" \
  "v_exp_f32 v88, v88\n\t" \
  "v_exp_f32 v89, v89\n\t" \
  "v_exp_f32 v90, v90\n\t" \
  "v_exp_f32 v91, v91\n\t" \
  "v_cvt_pk_bf16_f32 v114, v84, v85\n\t" \
  "v_cvt_pk_bf16_f32 v115, v86, v87\n\t" \
  "v_mfma_f32_32x32x16_bf16 %[o1], v[220:223], v[124:127], %[o1]\n\t" \
  "v_exp_f32 v92, v92\n\t" \
  "v_exp_f32 v93, v93\n\t" \
  "v_exp_f32 v94, v94\n\t" \
  "v_exp_f32 v95, v95\n\t" \
  "v_cvt_pk_bf16_f32 v116, v88, v89\n\t" \
  "v_cvt_pk_bf16_f32 v117, v90, v91\n\t" \
  "global_load_dwordx4 v[224:227], %[voff], %[kb3] offset:0\n\t" \
  "global_load_dwordx4 v[228:231], %[voff], %[kb3] offset:1024\n\t" \
  "global_load_dwordx4 v[216:219], %[voff], %[vb2] offset:2048\n\t" \
  "global_load_dwordx4 v[220:223], %[voff], %[vb2] offset:3072\n\t" \
  "s_waitcnt vmcnt(28)\n\t" \
  "v_cvt_pk_bf16_f32 v118, v92, v93\n\t" \
  "v_cvt_pk_bf16_f32 v119, v94, v95\n\t" \
  "v_mfma_f32_32x32x16_bf16 v[80:95], v[240:243], v[64:67], 0\n\t" \
  "v_exp_f32 v96, v96\n\t" \
  "v_exp_f32 v97, v97\n\t" \
  "v_exp_f32 v98, v98\n\t" \
  "v_exp_f32 v99, v99\n\t" \
  "v_mfma_f32_32x32x16_bf16 %[o0], v[232:235], v[112:115], %[o0]\n\t" \
  "v_exp_f32 v100, v100\n\t" \
  "v_exp_f32 v101, v101\n\t" \
  "v_exp_f32 v102, v102\n\t" \
  "v_exp_f32 v103, v103\n\t" \
  "v_cvt_pk_bf16_f32 v120, v96, v97\n\t" \
  "v_cvt_pk_bf16_f32 v121, v98, v99\n\t" \
  "v_mfma_f32_32x32x16_bf16 v[80:95], v[244:247], v[68:71], v[80:95]\n\t" \
  "v_exp_f32 v104, v104\n\t" \
  "v_exp_f32 v105, v105\n\t" \
  "v_exp_f32 v106, v106\n\t" \
  "v_exp_f32 v107, v107\n\t" \
  "v_cvt_pk_bf16_f32 v122, v100, v101\n\t" \
  "v_cvt_pk_bf16_f32 v123, v102, v103\n\t" \
  "v_mfma_f32_32x32x16_bf16 %[o0], v[236:239], v[116:119], %[o0]\n\t" \
  "v_exp_f32 v108, v108\n\t" \
  "v_exp_f32 v109, v109\n\t" \
  "v_exp_f32 v110, v110\n\t" \
  "v_exp_f32 v111, v111\n\t" \
  "v_cvt_pk_bf16_f32 v124, v104, v105\n\t" \
  "v_cvt_pk_bf16_f32 v125, v106, v107\n\t" \
  "v_cvt_pk_bf16_f32 v126, v108, v109\n\t" \
  "v_cvt_pk_bf16_f32 v127, v110, v111\n\t" \
  "v_mfma_f32_32x32x16_bf16 v[96:111], v[240:243], v[72:75], 0\n\t" \
  "v_exp_f32 v80, v80\n\t" \
  "v_exp_f32 v81, v81\n\t" \
  "v_exp_f32 v82, v82\n\t" \
  "v_exp_f32 v83, v83\n\t" \
  "v_mfma_f32_32x32x16_bf16 %[o1], v[232:235], v[120:123], %[o1]\n\t" \
  "v_exp_f32 v84, v84\n\t" \
  "v_exp_f32 v85, v85\n\t" \
  "v_exp_f32 v86, v86\n\t" \
  "v_exp_f32 v87, v87\n\t" \
  "v_cvt_pk_bf16_f32 v112, v80, v81\n\t" \
  "v_cvt_pk_bf16_f32 v113, v82, v83\n\t" \
  "v_mfma_f32_32x32x16_bf16 v[96:111], v[244:247], v[76:79], v[96:111]\n\t" \
  "v_exp_f32 v88, v88\n\t" \
  "v_exp_f32 v89, v89\n\t" \
  "v_exp_f32 v90, v90\n\t" \
  "v_exp_f32 v91, v91\n\t" \
  "v_cvt_pk_bf16_f32 v114, v84, v85\n\t" \
  "v_cvt_pk_bf16_f32 v115, v86, v87\n\t" \
  "v_mfma_f32_32x32x16_bf16 %[o1], v[236:239], v[124:127], %[o1]\n\t" \
  "v_exp_f32 v92, v92\n\t" \
  "v_exp_f32 v93, v93\n\t" \
  "v_exp_f32 v94, v94\n\t" \
  "v_exp_f32 v95, v95\n\t" \
  "v_cvt_pk_bf16_f32 v116, v88, v89\n\t" \
  "v_cvt_pk_bf16_f32 v117, v90, v91\n\t" \
  "global_load_dwordx4 v[240:243], %[voff], %[kb3] offset:2048\n\t" \
  "global_load_dwordx4 v[244:247], %[voff], %[kb3] offset:3072\n\t" \
  "global_load_dwordx4 v[232:235], %[voff], %[vb3] offset:0\n\t" \
  "global_load_dwordx4 v[236:239], %[voff], %[vb3] offset:1024\n\t" \
  "s_waitcnt vmcnt(28)\n\t" \
  "v_cvt_pk_bf16_f32 v118, v92, v93\n\t" \
  "v_cvt_pk_bf16_f32 v119, v94, v95\n\t" \
  "v_mfma_f32_32x32x16_bf16 v[80:95], v[128:131], v[64:67], 0\n\t" \
  "v_exp_f32 v96, v96\n\t" \
  "v_exp_f32 v97, v97\n\t" \
  "v_exp_f32 v98, v98\n\t" \
  "v_exp_f32 v99, v99\n\t" \
  "v_mfma_f32_32x32x16_bf16 %[o0], v[248:251], v[112:115], %[o0]\n\t" \
  "v_exp_f32 v100, v100\n\t" \
  "v_exp_f32 v101, v101\n\t" \
  "v_exp_f32 v102, v102\n\t" \
  "v_exp_f32 v103, v103\n\t" \
  "v_cvt_pk_bf16_f32 v120, v96, v97\n\t" \
  "v_cvt_pk_bf16_f32 v121, v98, v99\n\t" \
  "v_mfma_f32_32x32x16_bf16 v[80:95], v[132:135], v[68:71], v[80:95]\n\t" \
  "v_exp_f32 v104, v104\n\t" \
  "v_exp_f32 v105, v105\n\t" \
  "v_exp_f32 v106, v106\n\t" \
  "v_exp_f32 v107, v107\n\t" \
  "v_cvt_pk_bf16_f32 v122, v100, v101\n\t" \
  "v_cvt_pk_bf16_f32 v123, v102, v103\n\t" \
  "v_mfma_f32_32x32x16_bf16 %[o0], v[252:255], v[116:119], %[o0]\n\t" \
  "v_exp_f32 v108, v108\n\t" \
  "v_exp_f32 v109, v109\n\t" \
  "v_exp_f32 v110, v110\n\t" \
  "v_exp_f32 v111, v111\n\t" \
  "v_cvt_pk_bf16_f32 v124, v104, v105\n\t" \
  "v_cvt_pk_bf16_f32 v125, v106, v107\n\t" \
  "s_sub_u32 s42, s42, 1\n\t" \
  "s_cmp_lg_u32 s42, 0\n\t" \
  "s_cbranch_scc1 1b\n\t" \
  "v_cvt_pk_bf16_f32 v126, v108, v109\n\t" \
  "v_cvt_pk_bf16_f32 v127, v110, v111\n\t" \
  "v_mfma_f32_32x32x16_bf16 v[96:111], v[128:131], v[72:75], 0\n\t" \
  "v_exp_f32 v80, v80\n\t" \
  "v_exp_f32 v81, v81\n\t" \
  "v_exp_f32 v82, v82\n\t" \
  "v_exp_f32 v83, v83\n\t" \
  "v_mfma_f32_32x32x16_bf16 %[o1], v[248:251], v[120:123], %[o1]\n\t" \
  "v_exp_f32 v84, v84\n\t" \
  "v_exp_f32 v85, v85\n\t" \
  "v_exp_f32 v86, v86\n\t" \
  "v_exp_f32 v87, v87\n\t" \
  "v_cvt_pk_bf16_f32 v112, v80, v81\n\t" \
  "v_cvt_pk_bf16_f32 v113, v82, v83\n\t" \
  "v_mfma_f32_32x32x16_bf16 v[96:111], v[132:135], v[76:79], v[96:111]\n\t" \
  "v_exp_f32 v88, v88\n\t" \
  "v_exp_f32 v89, v89\n\t" \
  "v_exp_f32 v90, v90\n\t" \
  "v_exp_f32 v91, v91\n\t" \
  "v_cvt_pk_bf16_f32 v114, v84, v85\n\t" \
  "v_cvt_pk_bf16_f32 v115, v86, v87\n\t" \
  "v_mfma_f32_32x32x16_bf16 %[o1], v[252:255], v[124:127], %[o1]\n\t" \
  "v_exp_f32 v92, v92\n\t" \
  "v_exp_f32 v93, v93\n\t" \
  "v_exp_f32 v94, v94\n\t" \
  "v_exp_f32 v95, v95\n\t" \
  "v_cvt_pk_bf16_f32 v116, v88, v89\n\t" \
  "v_cvt_pk_bf16_f32 v117, v90, v91\n\t" \
  "global_load_dwordx4 v[248:251], %[voff], %[vb3] offset:2048\n\t" \
  "global_load_dwordx4 v[252:255], %[voff], %[vb3] offset:3072\n\t" \
  "s_waitcnt vmcnt(26)\n\t" \
  "v_cvt_pk_bf16_f32 v118, v92, v93\n\t" \
  "v_cvt_pk_bf16_f32 v119, v94, v95\n\t" \
  "v_mfma_f32_32x32x16_bf16 v[80:95], v[144:147], v[64:67], 0\n\t" \
  "v_exp_f32 v96, v96\n\t" \
  "v_exp_f32 v97, v97\n\t" \
  "v_exp_f32 v98, v98\n\t" \
  "v_exp_f32 v99, v99\n\t" \
  "v_mfma_f32_32x32x16_bf16 %[o0], v[136:139], v[112:115], %[o0]\n\t" \
  "v_exp_f32 v100, v100\n\t" \
  "v_exp_f32 v101, v101\n\t" \
  "v_exp_f32 v102, v102\n\t" \
  "v_exp_f32 v103, v103\n\t" \
  "v_cvt_pk_bf16_f32 v120, v96, v97\n\t" \
  "v_cvt_pk_bf16_f32 v121, v98, v99\n\t" \
  "v_mfma_f32_32x32x16_bf16 v[80:95], v[148:151], v[68:71], v[80:95]\n\t" \
  "v_exp_f32 v104, v104\n\t" \
  "v_exp_f32 v105, v105\n\t" \
  "v_exp_f32 v106, v106\n\t" \
  "v_exp_f32 v107, v107\n\t" \
  "v_cvt_pk_bf16_f32 v122, v100, v101\n\t" \
  "v_cvt_pk_bf16_f32 v123, v102, v103\n\t" \
  "v_mfma_f32_32x32x16_bf16 %[o0], v[140:143], v[116:119], %[o0]\n\t" \
  "v_exp_f32 v108, v108\n\t" \
  "v_exp_f32 v109, v109\n\t" \
  "v_exp_f32 v110, v110\n\t" \
  "v_exp_f32 v111, v111\n\t" \
  "v_cvt_pk_bf16_f32 v124, v104, v105\n\t" \
  "v_cvt_pk_bf16_f32 v125, v106, v107\n\t" \
  "v_cvt_pk_bf16_f32 v126, v108, v109\n\t" \
  "v_cvt_pk_bf16_f32 v127, v110, v111\n\t" \
  "v_mfma_f32_32x32x16_bf16 v[96:111], v[144:147], v[72:75], 0\n\t" \
  "v_exp_f32 v80, v80\n\t" \
  "v_exp_f32 v81, v81\n\t" \
  "v_exp_f32 v82, v82\n\t" \
  "v_exp_f32 v83, v83\n\t" \
  "v_mfma_f32_32x32x16_bf16 %[o1], v[136:139], v[120:123], %[o1]\n\t" \
  "v_exp_f32 v84, v84\n\t" \
  "v_exp_f32 v85, v85\n\t" \
  "v_exp_f32 v86, v86\n\t" \
  "v_exp_f32 v87, v87\n\t" \
  "v_cvt_pk_bf16_f32 v112, v80, v81\n\t" \
  "v_cvt_pk_bf16_f32 v113, v82, v83\n\t" \
  "v_mfma_f32_32x32x16_bf16 v[96:111], v[148:151], v[76:79], v[96:111]\n\t" \
  "v_exp_f32 v88, v88\n\t" \
  "v_exp_f32 v89, v89\n\t" \
  "v_exp_f32 v90, v90\n\t" \
  "v_exp_f32 v91, v91\n\t" \
  "v_cvt_pk_bf16_f32 v114, v84, v85\n\t" \
  "v_cvt_pk_bf16_f32 v115, v86, v87\n\t" \
  "v_mfma_f32_32x32x16_bf16 %[o1], v[140:143], v[124:127], %[o1]\n\t" \
  "v_exp_f32 v92, v92\n\t" \
  "v_exp_f32 v93, v93\n\t" \
  "v_exp_f32 v94, v94\n\t" \
  "v_exp_f32 v95, v95\n\t" \
  "v_cvt_pk_bf16_f32 v116, v88, v89\n\t" \
  "v_cvt_pk_bf16_f32 v117, v90, v91\n\t" \
  "s_waitcnt vmcnt(22)\n\t" \
  "v_cvt_pk_bf16_f32 v118, v92, v93\n\t" \
  "v_cvt_pk_bf16_f32 v119, v94, v95\n\t" \
  "v_mfma_f32_32x32x16_bf16 v[80:95], v[160:163], v[64:67], 0\n\t" \
  "v_exp_f32 v96, v96\n\t" \
  "v_exp_f32 v97, v97\n\t" \
  "v_exp_f32 v98, v98\n\t" \
  "v_exp_f32 v99, v99\n\t" \
  "v_mfma_f32_32x32x16_bf16 %[o0], v[152:155], v[112:115], %[o0]\n\t" \
  "v_exp_f32 v100, v100\n\t" \
  "v_exp_f32 v101, v101\n\t" \
  "v_exp_f32 v102, v102\n\t" \
  "v_exp_f32 v103, v103\n\t" \
  "v_cvt_pk_bf16_f32 v120, v96, v97\n\t" \
  "v_cvt_pk_bf16_f32 v121, v98, v99\n\t" \
  "v_mfma_f32_32x32x16_bf16 v[80:95], v[164:167], v[68:71], v[80:95]\n\t" \
  "v_exp_f32 v104, v104\n\t" \
  "v_exp_f32 v105, v105\n\t" \
  "v_exp_f32 v106, v106\n\t" \
  "v_exp_f32 v107, v107\n\t" \
  "v_cvt_pk_bf16_f32 v122, v100, v101\n\t" \
  "v_cvt_pk_bf16_f32 v123, v102, v103\n\t" \
  "v_mfma_f32_32x32x16_bf16 %[o0], v[156:159], v[116:119], %[o0]\n\t" \
  "v_exp_f32 v108, v108\n\t" \
  "v_exp_f32 v109, v109\n\t" \
  "v_exp_f32 v110, v110\n\t" \
  "v_exp_f32 v111, v111\n\t" \
  "v_cvt_pk_bf16_f32 v124, v104, v105\n\t" \
  "v_cvt_pk_bf16_f32 v125, v106, v107\n\t" \
  "v_cvt_pk_bf16_f32 v126, v108, v109\n\t" \
  "v_cvt_pk_bf16_f32 v127, v110, v111\n\t" \
  "v_mfma_f32_32x32x16_bf16 v[96:111], v[160:163], v[72:75], 0\n\t" \
  "v_exp_f32 v80, v80\n\t" \
  "v_exp_f32 v81, v81\n\t" \
  "v_exp_f32 v82, v82\n\t" \
  "v_exp_f32 v83, v83\n\t" \
  "v_mfma_f32_32x32x16_bf16 %[o1], v[152:155], v[120:123], %[o1]\n\t" \
  "v_exp_f32 v84, v84\n\t" \
  "v_exp_f32 v85, v85\n\t" \
  "v_exp_f32 v86, v86\n\t" \
  "v_exp_f32 v87, v87\n\t" \
  "v_cvt_pk_bf16_f32 v112, v80, v81\n\t" \
  "v_cvt_pk_bf16_f32 v113, v82, v83\n\t" \
  "v_mfma_f32_32x32x16_bf16 v[96:111], v[164:167], v[76:79], v[96:111]\n\t" \
  "v_exp_f32 v88, v88\n\t" \
  "v_exp_f32 v89, v89\n\t" \
  "v_exp_f32 v90, v90\n\t" \
  "v_exp_f32 v91, v91\n\t" \
  "v_cvt_pk_bf16_f32 v114, v84, v85\n\t" \
  "v_cvt_pk_bf16_f32 v115, v86, v87\n\t" \
  "v_mfma_f32_32x32x16_bf16 %[o1], v[156:159], v[124:127], %[o1]\n\t" \
  "v_exp_f32 v92, v92\n\t" \
  "v_exp_f32 v93, v93\n\t" \
  "v_exp_f32 v94, v94\n\t" \
  "v_exp_f32 v95, v95\n\t" \
  "v_cvt_pk_bf16_f32 v116, v88, v89\n\t" \
  "v_cvt_pk_bf16_f32 v117, v90, v91\n\t" \
  "s_waitcnt vmcnt(18)\n\t" \
  "v_cvt_pk_bf16_f32 v118, v92, v93\n\t" \
  "v_cvt_pk_bf16_f32 v119, v94, v95\n\t" \
  "v_mfma_f32_32x32x16_bf16 v[80:95], v[176:179], v[64:67], 0\n\t" \
  "v_exp_f32 v96, v96\n\t" \
  "v_exp_f32 v97, v97\n\t" \
  "v_exp_f32 v98, v98\n\t" \
  "v_exp_f32 v99, v99\n\t" \
  "v_mfma_f32_32x32x16_bf16 %[o0], v[168:171], v[112:115], %[o0]\n\t" \
  "v_exp_f32 v100, v100\n\t" \
  "v_exp_f32 v101, v101\n\t" \
  "v_exp_f32 v102, v102\n\t" \
  "v_exp_f32 v103, v103\n\t" \
  "v_cvt_pk_bf16_f32 v120, v96, v97\n\t" \
  "v_cvt_pk_bf16_f32 v121, v98, v99\n\t" \
  "v_mfma_f32_32x32x16_bf16 v[80:95], v[180:183], v[68:71], v[80:95]\n\t" \
  "v_exp_f32 v104, v104\n\t" \
  "v_exp_f32 v105, v105\n\t" \
  "v_exp_f32 v106, v106\n\t" \
  "v_exp_f32 v107, v107\n\t" \
  "v_cvt_pk_bf16_f32 v122, v100, v101\n\t" \
  "v_cvt_pk_bf16_f32 v123, v102, v103\n\t" \
  "v_mfma_f32_32x32x16_bf16 %[o0], v[172:175], v[116:119], %[o0]\n\t" \
  "v_exp_f32 v108, v108\n\t" \
  "v_exp_f32 v109, v109\n\t" \
  "v_exp_f32 v110, v110\n\t" \
  "v_exp_f32 v111, v111\n\t" \
  "v_cvt_pk_bf16_f32 v124, v104, v105\n\t" \
  "v_cvt_pk_bf16_f32 v125, v106, v107\n\t" \
  "v_cvt_pk_bf16_f32 v126, v108, v109\n\t" \
  "v_cvt_pk_bf16_f32 v127, v110, v111\n\t" \
  "v_mfma_f32_32x32x16_bf16 v[96:111], v[176:179], v[72:75], 0\n\t" \
  "v_exp_f32 v80, v80\n\t" \
  "v_exp_f32 v81, v81\n\t" \
  "v_exp_f32 v82, v82\n\t" \
  "v_exp_f32 v83, v83\n\t" \
  "v_mfma_f32_32x32x16_bf16 %[o1], v[168:171], v[120:123], %[o1]\n\t" \
  "v_exp_f32 v84, v84\n\t" \
  "v_exp_f32 v85, v85\n\t" \
  "v_exp_f32 v86, v86\n\t" \
  "v_exp_f32 v87, v87\n\t" \
  "v_cvt_pk_bf16_f32 v112, v80, v81\n\t" \
  "v_cvt_pk_bf16_f32 v113, v82, v83\n\t" \
  "v_mfma_f32_32x32x16_bf16 v[96:111], v[180:183], v[76:79], v[96:111]\n\t" \
  "v_exp_f32 v88, v88\n\t" \
  "v_exp_f32 v89, v89\n\t" \
  "v_exp_f32 v90, v90\n\t" \
  "v_exp_f32 v91, v91\n\t" \
  "v_cvt_pk_bf16_f32 v114, v84, v85\n\t" \
  "v_cvt_pk_bf16_f32 v115, v86, v87\n\t" \
  "v_mfma_f32_32x32x16_bf16 %[o1], v[172:175], v[124:127], %[o1]\n\t" \
  "v_exp_f32 v92, v92\n\t" \
  "v_exp_f32 v93, v93\n\t" \
  "v_exp_f32 v94, v94\n\t" \
  "v_exp_f32 v95, v95\n\t" \
  "v_cvt_pk_bf16_f32 v116, v88, v89\n\t" \
  "v_cvt_pk_bf16_f32 v117, v90, v91\n\t" \
  "s_waitcnt vmcnt(14)\n\t" \
  "v_cvt_pk_bf16_f32 v118, v92, v93\n\t" \
  "v_cvt_pk_bf16_f32 v119, v94, v95\n\t" \
  "v_mfma_f32_32x32x16_bf16 v[80:95], v[192:195], v[64:67], 0\n\t" \
  "v_exp_f32 v96, v96\n\t" \
  "v_exp_f32 v97, v97\n\t" \
  "v_exp_f32 v98, v98\n\t" \
  "v_exp_f32 v99, v99\n\t" \
  "v_mfma_f32_32x32x16_bf16 %[o0], v[184:187], v[112:115], %[o0]\n\t" \
  "v_exp_f32 v100, v100\n\t" \
  "v_exp_f32 v101, v101\n\t" \
  "v_exp_f32 v102, v102\n\t" \
  "v_exp_f32 v103, v103\n\t" \
  "v_cvt_pk_bf16_f32 v120, v96, v97\n\t" \
  "v_cvt_pk_bf16_f32 v121, v98, v99\n\t" \
  "v_mfma_f32_32x32x16_bf16 v[80:95], v[196:199], v[68:71], v[80:95]\n\t" \
  "v_exp_f32 v104, v104\n\t" \
  "v_exp_f32 v105, v105\n\t" \
  "v_exp_f32 v106, v106\n\t" \
  "v_exp_f32 v107, v107\n\t" \
  "v_cvt_pk_bf16_f32 v122, v100, v101\n\t" \
  "v_cvt_pk_bf16_f32 v123, v102, v103\n\t" \
  "v_mfma_f32_32x32x16_bf16 %[o0], v[188:191], v[116:119], %[o0]\n\t" \
  "v_exp_f32 v108, v108\n\t" \
  "v_exp_f32 v109, v109\n\t" \
  "v_exp_f32 v110, v110\n\t" \
  "v_exp_f32 v111, v111\n\t" \
  "v_cvt_pk_bf16_f32 v124, v104, v105\n\t" \
  "v_cvt_pk_bf16_f32 v125, v106, v107\n\t" \
  "v_cvt_pk_bf16_f32 v126, v108, v109\n\t" \
  "v_cvt_pk_bf16_f32 v127, v110, v111\n\t" \
  "v_mfma_f32_32x32x16_bf16 v[96:111], v[192:195], v[72:75], 0\n\t" \
  "v_exp_f32 v80, v80\n\t" \
  "v_exp_f32 v81, v81\n\t" \
  "v_exp_f32 v82, v82\n\t" \
  "v_exp_f32 v83, v83\n\t" \
  "v_mfma_f32_32x32x16_bf16 %[o1], v[184:187], v[120:123], %[o1]\n\t" \
  "v_exp_f32 v84, v84\n\t" \
  "v_exp_f32 v85, v85\n\t" \
  "v_exp_f32 v86, v86\n\t" \
  "v_exp_f32 v87, v87\n\t" \
  "v_cvt_pk_bf16_f32 v112, v80, v81\n\t" \
  "v_cvt_pk_bf16_f32 v113, v82, v83\n\t" \
  "v_mfma_f32_32x32x16_bf16 v[96:111], v[196:199], v[76:79], v[96:111]\n\t" \
  "v_exp_f32 v88, v88\n\t" \
  "v_exp_f32 v89, v89\n\t" \
  "v_exp_f32 v90, v90\n\t" \
  "v_exp_f32 v91, v91\n\t" \
  "v_cvt_pk_bf16_f32 v114, v84, v85\n\t" \
  "v_cvt_pk_bf16_f32 v115, v86, v87\n\t" \
  "v_mfma_f32_32x32x16_bf16 %[o1], v[188:191], v[124:127], %[o1]\n\t" \
  "v_exp_f32 v92, v92\n\t" \
  "v_exp_f32 v93, v93\n\t" \
  "v_exp_f32 v94, v94\n\t" \
  "v_exp_f32 v95, v95\n\t" \
  "v_cvt_pk_bf16_f32 v116, v88, v89\n\t" \
  "v_cvt_pk_bf16_f32 v117, v90, v91\n\t" \
  "s_waitcnt vmcnt(10)\n\t" \
  "v_cvt_pk_bf16_f32 v118, v92, v93\n\t" \
  "v_cvt_pk_bf16_f32 v119, v94, v95\n\t" \
  "v_mfma_f32_32x32x16_bf16 v[80:95], v[208:211], v[64:67], 0\n\t" \
  "v_exp_f32 v96, v96\n\t" \
  "v_exp_f32 v97, v97\n\t" \
  "v_exp_f32 v98, v98\n\t" \
  "v_exp_f32 v99, v99\n\t" \
  "v_mfma_f32_32x32x16_bf16 %[o0], v[200:203], v[112:115], %[o0]\n\t" \
  "v_exp_f32 v100, v100\n\t" \
  "v_exp_f32 v101, v101\n\t" \
  "v_exp_f32 v102, v102\n\t" \
  "v_exp_f32 v103, v103\n\t" \
  "v_cvt_pk_bf16_f32 v120, v96, v97\n\t" \
  "v_cvt_pk_bf16_f32 v121, v98, v99\n\t" \
  "v_mfma_f32_32x32x16_bf16 v[80:95], v[212:215], v[68:71], v[80:95]\n\t" \
  "v_exp_f32 v104, v104\n\t" \
  "v_exp_f32 v105, v105\n\t" \
  "v_exp_f32 v106, v106\n\t" \
  "v_exp_f32 v107, v107\n\t" \
  "v_cvt_pk_bf16_f32 v122, v100, v101\n\t" \
  "v_cvt_pk_bf16_f32 v123, v102, v103\n\t" \
  "v_mfma_f32_32x32x16_bf16 %[o0], v[204:207], v[116:119], %[o0]\n\t" \
  "v_exp_f32 v108, v108\n\t" \
  "v_exp_f32 v109, v109\n\t" \
  "v_exp_f32 v110, v110\n\t" \
  "v_exp_f32 v111, v111\n\t" \
  "v_cvt_pk_bf16_f32 v124, v104, v105\n\t" \
  "v_cvt_pk_bf16_f32 v125, v106, v107\n\t" \
  "v_cvt_pk_bf16_f32 v126, v108, v109\n\t" \
  "v_cvt_pk_bf16_f32 v127, v110, v111\n\t" \
  "v_mfma_f32_32x32x16_bf16 v[96:111], v[208:211], v[72:75], 0\n\t" \
  "v_exp_f32 v80, v80\n\t" \
  "v_exp_f32 v81, v81\n\t" \
  "v_exp_f32 v82, v82\n\t" \
  "v_exp_f32 v83, v83\n\t" \
  "v_mfma_f32_32x32x16_bf16 %[o1], v[200:203], v[120:123], %[o1]\n\t" \
  "v_exp_f32 v84, v84\n\t" \
  "v_exp_f32 v85, v85\n\t" \
  "v_exp_f32 v86, v86\n\t" \
  "v_exp_f32 v87, v87\n\t" \
  "v_cvt_pk_bf16_f32 v112, v80, v81\n\t" \
  "v_cvt_pk_bf16_f32 v113, v82, v83\n\t" \
  "v_mfma_f32_32x32x16_bf16 v[96:111], v[212:215], v[76:79], v[96:111]\n\t" \
  "v_exp_f32 v88, v88\n\t" \
  "v_exp_f32 v89, v89\n\t" \
  "v_exp_f32 v90, v90\n\t" \
  "v_exp_f32 v91, v91\n\t" \
  "v_cvt_pk_bf16_f32 v114, v84, v85\n\t" \
  "v_cvt_pk_bf16_f32 v115, v86, v87\n\t" \
  "v_mfma_f32_32x32x16_bf16 %[o1], v[204:207], v[124:127], %[o1]\n\t" \
  "v_exp_f32 v92, v92\n\t" \
  "v_exp_f32 v93, v93\n\t" \
  "v_exp_f32 v94, v94\n\t" \
  "v_exp_f32 v95, v95\n\t" \
  "v_cvt_pk_bf16_f32 v116, v88, v89\n\t" \
  "v_cvt_pk_bf16_f32 v117, v90, v91\n\t" \
  "s_waitcnt vmcnt(6)\n\t" \
  "v_cvt_pk_bf16_f32 v118, v92, v93\n\t" \
  "v_cvt_pk_bf16_f32 v119, v94, v95\n\t" \
  "v_mfma_f32_32x32x16_bf16 v[80:95], v[224:227], v[64:67], 0\n\t" \
  "v_exp_f32 v96, v96\n\t" \
  "v_exp_f32 v97, v97\n\t" \
  "v_exp_f32 v98, v98\n\t" \
  "v_exp_f32 v99, v99\n\t" \
  "v_mfma_f32_32x32x16_bf16 %[o0], v[216:219], v[112:115], %[o0]\n\t" \
  "v_exp_f32 v100, v100\n\t" \
  "v_exp_f32 v101, v101\n\t" \
  "v_exp_f32 v102, v102\n\t" \
  "v_exp_f32 v103, v103\n\t" \
  "v_cvt_pk_bf16_f32 v120, v96, v97\n\t" \
  "v_cvt_pk_bf16_f32 v121, v98, v99\n\t" \
  "v_mfma_f32_32x32x16_bf16 v[80:95], v[228:231], v[68:71], v[80:95]\n\t" \
  "v_exp_f32 v104, v104\n\t" \
  "v_exp_f32 v105, v105\n\t" \
  "v_exp_f32 v106, v106\n\t" \
  "v_exp_f32 v107, v107\n\t" \
  "v_cvt_pk_bf16_f32 v122, v100, v101\n\t" \
  "v_cvt_pk_bf16_f32 v123, v102, v103\n\t" \
  "v_mfma_f32_32x32x16_bf16 %[o0], v[220:223], v[116:119], %[o0]\n\t" \
  "v_exp_f32 v108, v108\n\t" \
  "v_exp_f32 v109, v109\n\t" \
  "v_exp_f32 v110, v110\n\t" \
  "v_exp_f32 v111, v111\n\t" \
  "v_cvt_pk_bf16_f32 v124, v104, v105\n\t" \
  "v_cvt_pk_bf16_f32 v125, v106, v107\n\t" \
  "v_cvt_pk_bf16_f32 v126, v108, v109\n\t" \
  "v_cvt_pk_bf16_f32 v127, v110, v111\n\t" \
  "v_mfma_f32_32x32x16_bf16 v[96:111], v[224:227], v[72:75], 0\n\t" \
  "v_exp_f32 v80, v80\n\t" \
  "v_exp_f32 v81, v81\n\t" \
  "v_exp_f32 v82, v82\n\t" \
  "v_exp_f32 v83, v83\n\t" \
  "v_mfma_f32_32x32x16_bf16 %[o1], v[216:219], v[120:123], %[o1]\n\t" \
  "v_exp_f32 v84, v84\n\t" \
  "v_exp_f32 v85, v85\n\t" \
  "v_exp_f32 v86, v86\n\t" \
  "v_exp_f32 v87, v87\n\t" \
  "v_cvt_pk_bf16_f32 v112, v80, v81\n\t" \
  "v_cvt_pk_bf16_f32 v113, v82, v83\n\t" \
  "v_mfma_f32_32x32x16_bf16 v[96:111], v[228:231], v[76:79], v[96:111]\n\t" \
  "v_exp_f32 v88, v88\n\t" \
  "v_exp_f32 v89, v89\n\t" \
  "v_exp_f32 v90, v90\n\t" \
  "v_exp_f32 v91, v91\n\t" \
  "v_cvt_pk_bf16_f32 v114, v84, v85\n\t" \
  "v_cvt_pk_bf16_f32 v115, v86, v87\n\t" \
  "v_mfma_f32_32x32x16_bf16 %[o1], v[220:223], v[124:127], %[o1]\n\t" \
  "v_exp_f32 v92, v92\n\t" \
  "v_exp_f32 v93, v93\n\t" \
  "v_exp_f32 v94, v94\n\t" \
  "v_exp_f32 v95, v95\n\t" \
  "v_cvt_pk_bf16_f32 v116, v88, v89\n\t" \
  "v_cvt_pk_bf16_f32 v117, v90, v91\n\t" \
  "s_waitcnt vmcnt(2)\n\t" \
  "v_cvt_pk_bf16_f32 v118, v92, v93\n\t" \
  "v_cvt_pk_bf16_f32 v119, v94, v95\n\t" \
  "v_mfma_f32_32x32x16_bf16 v[80:95], v[240:243], v[64:67], 0\n\t" \
  "v_exp_f32 v96, v96\n\t" \
  "v_exp_f32 v97, v97\n\t" \
  "v_exp_f32 v98, v98\n\t" \
  "v_exp_f32 v99, v99\n\t" \
  "v_mfma_f32_32x32x16_bf16 %[o0], v[232:235], v[112:115], %[o0]\n\t" \
  "v_exp_f32 v100, v100\n\t" \
  "v_exp_f32 v101, v101\n\t" \
  "v_exp_f32 v102, v102\n\t" \
  "v_exp_f32 v103, v103\n\t" \
  "v_cvt_pk_bf16_f32 v120, v96, v97\n\t" \
  "v_cvt_pk_bf16_f32 v121, v98, v99\n\t" \
  "v_mfma_f32_32x32x16_bf16 v[80:95], v[244:247], v[68:71], v[80:95]\n\t" \
  "v_exp_f32 v104, v104\n\t" \
  "v_exp_f32 v105, v105\n\t" \
  "v_exp_f32 v106, v106\n\t" \
  "v_exp_f32 v107, v107\n\t" \
  "v_cvt_pk_bf16_f32 v122, v100, v101\n\t" \
  "v_cvt_pk_bf16_f32 v123, v102, v103\n\t" \
  "v_mfma_f32_32x32x16_bf16 %[o0], v[236:239], v[116:119], %[o0]\n\t" \
  "v_exp_f32 v108, v108\n\t" \
  "v_exp_f32 v109, v109\n\t" \
  "v_exp_f32 v110, v110\n\t" \
  "v_exp_f32 v111, v111\n\t" \
  "v_cvt_pk_bf16_f32 v124, v104, v105\n\t" \
  "v_cvt_pk_bf16_f32 v125, v106, v107\n\t" \
  "v_cvt_pk_bf16_f32 v126, v108, v109\n\t" \
  "v_cvt_pk_bf16_f32 v127, v110, v111\n\t" \
  "v_mfma_f32_32x32x16_bf16 v[96:111], v[240:243], v[72:75], 0\n\t" \
  "v_exp_f32 v80, v80\n\t" \
  "v_exp_f32 v81, v81\n\t" \
  "v_exp_f32 v82, v82\n\t" \
  "v_exp_f32 v83, v83\n\t" \
  "v_mfma_f32_32x32x16_bf16 %[o1], v[232:235], v[120:123], %[o1]\n\t" \
  "v_exp_f32 v84, v84\n\t" \
  "v_exp_f32 v85, v85\n\t" \
  "v_exp_f32 v86, v86\n\t" \
  "v_exp_f32 v87, v87\n\t" \
  "v_cvt_pk_bf16_f32 v112, v80, v81\n\t" \
  "v_cvt_pk_bf16_f32 v113, v82, v83\n\t" \
  "v_mfma_f32_32x32x16_bf16 v[96:111], v[244:247], v[76:79], v[96:111]\n\t" \
  "v_exp_f32 v88, v88\n\t" \
  "v_exp_f32 v89, v89\n\t" \
  "v_exp_f32 v90, v90\n\t" \
  "v_exp_f32 v91, v91\n\t" \
  "v_cvt_pk_bf16_f32 v114, v84, v85\n\t" \
  "v_cvt_pk_bf16_f32 v115, v86, v87\n\t" \
  "v_mfma_f32_32x32x16_bf16 %[o1], v[236:239], v[124:127], %[o1]\n\t" \
  "v_exp_f32 v92, v92\n\t" \
  "v_exp_f32 v93, v93\n\t" \
  "v_exp_f32 v94, v94\n\t" \
  "v_exp_f32 v95, v95\n\t" \
  "v_cvt_pk_bf16_f32 v116, v88, v89\n\t" \
  "v_cvt_pk_bf16_f32 v117, v90, v91\n\t" \
  "s_waitcnt vmcnt(0)\n\t" \
  "v_cvt_pk_bf16_f32 v118, v92, v93\n\t" \
  "v_cvt_pk_bf16_f32 v119, v94, v95\n\t" \
  "v_exp_f32 v96, v96\n\t" \
  "v_exp_f32 v97, v97\n\t" \
  "v_exp_f32 v98, v98\n\t" \
  "v_exp_f32 v99, v99\n\t" \
  "v_mfma_f32_32x32x16_bf16 %[o0], v[248:251], v[112:115], %[o0]\n\t" \
  "v_exp_f32 v100, v100\n\t" \
  "v_exp_f32 v101, v101\n\t" \
  "v_exp_f32 v102, v102\n\t" \
  "v_exp_f32 v103, v103\n\t" \
  "v_cvt_pk_bf16_f32 v120, v96, v97\n\t" \
  "v_cvt_pk_bf16_f32 v121, v98, v99\n\t" \
  "v_exp_f32 v104, v104\n\t" \
  "v_exp_f32 v105, v105\n\t" \
  "v_exp_f32 v106, v106\n\t" \
  "v_exp_f32 v107, v107\n\t" \
  "v_cvt_pk_bf16_f32 v122, v100, v101\n\t" \
  "v_cvt_pk_bf16_f32 v123, v102, v103\n\t" \
  "v_mfma_f32_32x32x16_bf16 %[o0], v[252:255], v[116:119], %[o0]\n\t" \
  "v_exp_f32 v108, v108\n\t" \
  "v_exp_f32 v109, v109\n\t" \
  "v_exp_f32 v110, v110\n\t" \
  "v_exp_f32 v111, v111\n\t" \
  "v_cvt_pk_bf16_f32 v124, v104, v105\n\t" \
  "v_cvt_pk_bf16_f32 v125, v106, v107\n\t" \
  "v_cvt_pk_bf16_f32 v126, v108, v109\n\t" \
  "v_cvt_pk_bf16_f32 v127, v110, v111\n\t" \
  "v_mfma_f32_32x32x16_bf16 %[o1], v[248:251], v[120:123], %[o1]\n\t" \
  "s_nop 0\n\t" \
  "v_mfma_f32_32x32x16_bf16 %[o1], v[252:255], v[124:127], %[o1]\n\t" \
  "s_nop 15\n\t"
#define ATTN_CLOBBERS "v64", "v65", "v66", "v67", "v68", "v69", "v70", "v71", "v72", "v73", "v74", "v75", "v76", "v77", "v78", "v79", "v80", "v81", "v82", "v83", "v84", "v85", "v86", "v87", "v88", "v89", "v90", "v91", "v92", "v93", "v94", "v95", "v96", "v97", "v98", "v99", "v100", "v101", "v102", "v103", "v104", "v105", "v106", "v107", "v108", "v109", "v110", "v111", "v112", "v113", "v114", "v115", "v116", "v117", "v118", "v119", "v120", "v121", "v122", "v123", "v124", "v125", "v126", "v127", "v128", "v129", "v130", "v131", "v132", "v133", "v134", "v135", "v136", "v137", "v138", "v139", "v140", "v141", "v142", "v143", "v144", "v145", "v146", "v147", "v148", "v149", "v150", "v151", "v152", "v153", "v154", "v155", "v156", "v157", "v158", "v159", "v160", "v161", "v162", "v163", "v164", "v165", "v166", "v167", "v168", "v169", "v170", "v171", "v172", "v173", "v174", "v175", "v176", "v177", "v178", "v179", "v180", "v181", "v182", "v183", "v184", "v185", "v186", "v187", "v188", "v189", "v190", "v191", "v192", "v193", "v194", "v195", "v196", "v197", "v198", "v199", "v200", "v201", "v202", "v203", "v204", "v205", "v206", "v207", "v208", "v209", "v210", "v211", "v212", "v213", "v214", "v215", "v216", "v217", "v218", "v219", "v220", "v221", "v222", "v223", "v224", "v225", "v226", "v227", "v228", "v229", "v230", "v231", "v232", "v233", "v234", "v235", "v236", "v237", "v238", "v239", "v240", "v241", "v242", "v243", "v244", "v245", "v246", "v247", "v248", "v249", "v250", "v251", "v252", "v253", "v254", "v255"
#define AHEAD ((blockIdx.x & 7) * 2 + (idx & 1))
#define AQBLK (idx >> 1)
constexpr int L_SB = 0, L_AR = 7 * 16384, L_MISC = L_AR + 5 * 8192, KB_LDS = L_MISC + 8192;
__host__ __device__ constexpr int L_EB(int j) { return (j % 12) * 8192; }
__host__ __device__ constexpr int L_EA(int j) { return (12 + j % 7) * 8192; }
__host__ __device__ constexpr int EMS(int j) { return j < 14 ? 2 * j : 2 * (j - 14) + 1; }
__host__ __device__ constexpr int EROW(int j) { return (EMS(j) >> 1) * 128 + (EMS(j) & 1) * 32; }
constexpr int L_CA = 2 * 16384, L_WO = 6 * 16384, L_AB = L_MISC, L_ST = L_MISC + 2048, L_SCR = L_MISC + 4096, L_UT = L_MISC + 5120, L_CNT = L_MISC + 6144;
#define GAS __attribute__((address_space(1)))
DI void st16_sc1(void* sbase, unsigned voff, bf16x8 v) {
  asm volatile("global_store_dwordx4 %0, %1, %2 sc1\n\ts_nop 1" ::"v"(voff), "v"(v), "s"(sbase) : "memory");
}
DI void st8_sc1(void* sbase, unsigned voff, f32x2 v) {
  asm volatile("global_store_dwordx2 %0, %1, %2 sc1" ::"v"(voff), "v"(v), "s"(sbase) : "memory");
}
DI void glds16x2_sc1(const void* sbase, unsigned voff, unsigned lds_base) {
  asm volatile("s_mov_b32 m0, %2\n\ts_nop 0\n\tglobal_load_lds_dwordx4 %0, %1 sc1\n\tglobal_load_lds_dwordx4 %0, %1 offset:1024 sc1"
               ::"v"(voff), "s"(sbase), "s"(lds_base) : "memory");
}
DI void glds16x4_sc1(const void* sbase, unsigned voff, unsigned lds_base) {
  asm volatile("s_mov_b32 m0, %2\n\ts_nop 0\n\tglobal_load_lds_dwordx4 %0, %1 sc1\n\tglobal_load_lds_dwordx4 %0, %1 offset:1024 sc1\n\t"
               "global_load_lds_dwordx4 %0, %1 offset:2048 sc1\n\tglobal_load_lds_dwordx4 %0, %1 offset:3072 sc1" ::"v"(voff), "s"(sbase), "s"(lds_base) : "memory");
}
template <int OFF> DI void gld16x2_sc1(const void* sbase, unsigned voff, bf16x8& a, bf16x8& b) {
  asm volatile("global_load_dwordx4 %0, %2, %3 offset:%4 sc1\n\tglobal_load_dwordx4 %1, %2, %3 offset:%5 sc1"
               : "=&v"(a), "=&v"(b) : "v"(voff), "s"(sbase), "i"(OFF), "i"(OFF + 1024) : "memory");
}
template <int N> DI void waitv_pin(bf16x8& a, bf16x8& b) { asm volatile("s_waitcnt vmcnt(%2)" : "+v"(a), "+v"(b) : "i"(N) : "memory"); }
DI void publish(unsigned* cnt, int lane) {
  if (lane < 14) __hip_atomic_fetch_add((GAS unsigned*)cnt + lane * 32, 1u, __ATOMIC_RELAXED, __HIP_MEMORY_SCOPE_AGENT);
}
DI void arrive_publish(LAS unsigned* lc, unsigned* cnt, int lane) {
  unsigned old = 0;
  if (lane == 0) old = __hip_atomic_fetch_add(lc, 1u, __ATOMIC_RELAXED, __HIP_MEMORY_SCOPE_WORKGROUP);
  if (__builtin_amdgcn_readfirstlane(old) == 3u) publish(cnt, lane);
}
DI void lds_wait(LAS unsigned* lc, unsigned want) {
  for (unsigned spins = 0; spins < 4000000u; ++spins) {
    if (__builtin_amdgcn_readfirstlane(__hip_atomic_load(lc, __ATOMIC_RELAXED, __HIP_MEMORY_SCOPE_WORKGROUP)) >= want) break;
    __builtin_amdgcn_s_sleep(1);
  }
  asm volatile("" ::: "memory");
}
DI void lds_wait4(LAS unsigned* lc, unsigned want) {
  for (unsigned spins = 0; spins < 1000000u; ++spins) {
    if (__builtin_amdgcn_readfirstlane(__hip_atomic_load(lc, __ATOMIC_RELAXED, __HIP_MEMORY_SCOPE_WORKGROUP)) >= want) break;
    __builtin_amdgcn_s_sleep(4);
  }
  asm volatile("" ::: "memory");
}
DI void poll_count(const unsigned* c, unsigned want) {
  for (unsigned spins = 0; spins < 400000u; ++spins) {
    const unsigned v = __builtin_amdgcn_readfirstlane(__hip_atomic_load((const GAS unsigned*)c, __ATOMIC_RELAXED, __HIP_MEMORY_SCOPE_AGENT));
    if (v >= want) break;
    __builtin_amdgcn_s_sleep(1);
  }
  asm volatile("" ::: "memory");
}

DI void silu_tile(const f32x16& acc, float rstd, float nm, lds_ptr ut, char* ws, unsigned hp) {
  f32x4 u4[4], t4[4];
#pragma unroll
  for (int gq = 0; gq < 4; ++gq) { u4[gq] = *(const LAS f32x4*)(ut + gq * 32); t4[gq] = *(const LAS f32x4*)(ut + 512 + gq * 32); }
  f32x16 v, e;
#pragma unroll
  for (int r = 0; r < 16; ++r) v[r] = fmaf(acc[r], rstd, fmaf(nm, u4[r >> 2][r & 3], t4[r >> 2][r & 3]));
  PIN(v);
#pragma unroll
  for (int r = 0; r < 16; ++r) e[r] = __builtin_amdgcn_exp2f(v[r]);
  PIN(e);
  e += 1.f;
  PIN(e);
#pragma unroll
  for (int r = 0; r < 16; ++r) e[r] = __builtin_amdgcn_rcpf(e[r]);
  PIN(e);
  v *= e;
  st16_sc1(ws, hp, pack8(v, 0)); st16_sc1(ws, hp + 1024, pack8(v, 1));
}

DI void silu_tile_s(const f32x16& acc, float rstd, float nm, lds_ptr ut, char* ws, unsigned hp) {
  f32x4 u4[4], t4[4];
#pragma unroll
  for (int gq = 0; gq < 4; ++gq) { u4[gq] = *(const LAS f32x4*)(ut + gq * 32); t4[gq] = *(const LAS f32x4*)(ut + 512 + gq * 32); }
  float v[16], e[16];
#pragma unroll
  for (int r = 0; r < 16; ++r) asm("v_fma_f32 %0, %1, %2, %3" : "=v"(e[r]) : "v"(nm), "v"(u4[r >> 2][r & 3]), "v"(t4[r >> 2][r & 3]));
#pragma unroll
  for (int r = 0; r < 16; ++r) asm("v_fma_f32 %0, %1, %2, %3" : "=v"(v[r]) : "v"(acc[r]), "v"(rstd), "v"(e[r]));
  SB();
#pragma unroll
  for (int r = 0; r < 16; ++r) asm volatile("v_exp_f32 %0, %1" : "=v"(e[r]) : "v"(v[r]));
#pragma unroll
  for (int r = 0; r < 16; ++r) asm volatile("v_add_f32 %0, 1.0, %0" : "+v"(e[r]));
#pragma unroll
  for (int r = 0; r < 16; ++r) asm volatile("v_rcp_f32 %0, %0" : "+v"(e[r]));
#pragma unroll
  for (int r = 0; r < 16; ++r) asm volatile("v_mul_f32 %0, %0, %1" : "+v"(v[r]) : "v"(e[r]));
  st16_sc1(ws, hp, __builtin_bit_cast(bf16x8, (u32x4){cvtpk(v[0], v[1]), cvtpk(v[2], v[3]), cvtpk(v[4], v[5]), cvtpk(v[6], v[7])}));
  st16_sc1(ws, hp + 1024, __builtin_bit_cast(bf16x8, (u32x4){cvtpk(v[8], v[9]), cvtpk(v[10], v[11]), cvtpk(v[12], v[13]), cvtpk(v[14], v[15])}));
}

__global__ __launch_bounds__(512, 2) void kb_fused(const u16* __restrict__ x, const u16* __restrict__ wo, const u16* __restrict__ w1,
                                                   const u16* __restrict__ w2, char* __restrict__ ws, u16* __restrict__ out) {
  extern __shared__ __attribute__((aligned(16))) char smem[];
  lds_ptr sm = (lds_ptr)smem;
  const unsigned lds0 = (unsigned)(uintptr_t)smem;
  const int tid = threadIdx.x, lane = tid & 63, wid = __builtin_amdgcn_readfirstlane(tid >> 6), h = lane >> 5;
  const int idx = blockIdx.x >> 3, g = (blockIdx.x & 7) * 2 + (idx >> 4), m = idx & 15;
  const int r2 = m >= 7, cb = m - 7 * r2;
  unsigned* cnts = (unsigned*)(ws + WS_FLAGS);
  const int head = AHEAD, qblk = AQBLK;
  const float* mod = (const float*)(ws + WS_MOD);
  if (wid >= 4) {
    const int lw = wid - 4;
    const unsigned l16 = lane * 16;
    if (tid < 264) *(LAS unsigned*)(sm + L_CNT + (tid - 256) * 4) = 0u;
    const unsigned boff64 = (unsigned)(((lane >> 5) * 4 + ((lane >> 2) & 3)) * (D * 2) + cb * 128 + ((lane >> 4) & 1) * 64 + (lane & 3) * 16);
    const unsigned boff128 = (unsigned)(((lw & 1) * 16 + ((lane >> 2) & 3)) * (F * 2) + m * 256 + (lane >> 4) * 64 + (lane & 3) * 16);
    auto W1 = [&](int st) {
      const int hs = 2 * st + (lw >> 1);
      const char* wb = (const char*)w1 + (size_t)hs * 32 * (F * 2);
#pragma unroll
      for (int i = 0; i < 4; ++i) glds16(wb + i * 4 * (F * 2), boff128, lds0 + L_SB + st * 16384 + lw * 4096 + i * 1024);
    };
    auto EB = [&](int j) {
      const char* wb = (const char*)w2 + (size_t)(EROW(j) + (lw & 1) * 64) * (D * 2);
#pragma unroll
      for (int i = 0; i < 4; ++i) glds16(wb + i * 8 * (D * 2), boff64, lds0 + L_EB(j) + (lw & 1) * 4096 + i * 1024);
    };
    auto EB4 = [&](int j) {
      const char* wb = (const char*)w2 + (size_t)(EROW(j) + (lw >> 1) * 64 + (lw & 1) * 16) * (D * 2);
      glds16(wb, boff64, lds0 + L_EB(j) + lw * 2048); glds16(wb + 8 * (D * 2), boff64, lds0 + L_EB(j) + lw * 2048 + 1024);
    };
    {
      const unsigned j = (unsigned)((lane & 16) ? WS_V : WS_K) + head * 131072 + ((qblk * 4 + lw) * 16 + (lane & 15)) * 128;
      if (lane < 32) asm volatile("s_mov_b32 m0, %2\n\ts_nop 0\n\tglobal_load_lds_dword %0, %1" ::"v"(j), "s"(ws), "s"(lds0 + L_SCR) : "memory");
    }
    __builtin_amdgcn_s_sleep(80);
    {
      constexpr int N1 = D * F * 2 / 128;
      int j = (blockIdx.x * 4 + lw) * 25 + (lane < 25 ? lane : 24);
      j = j < 2 * N1 ? j : 2 * N1 - 1;
      const char* src = j < N1 ? (const char*)w1 + j * 128 : (const char*)w2 + (j - N1) * 128;
      if (lane < 25) asm volatile("s_mov_b32 m0, %1\n\ts_nop 0\n\tglobal_load_lds_dword %0, off" ::"v"(src), "s"(lds0 + L_SCR) : "memory");
    }
    if (m < 14) {
#pragma unroll 1
      for (int i = 0; i < 7; ++i) {
        int p0 = i * 8 + lw * 2 + 7 * cb, p1 = p0 + 1;
        p0 -= p0 >= 56 ? 56 : 0; p1 -= p1 >= 56 ? 56 : 0;
        glds16((const char*)wo + (size_t)p0 * 8 * (D * 2), boff64, lds0 + L_WO + i * 8192 + lw * 2048);
        glds16((const char*)wo + (size_t)p1 * 8 * (D * 2), boff64, lds0 + L_WO + i * 8192 + lw * 2048 + 1024);
        __builtin_amdgcn_s_sleep(8);
      }
      W1(0);
      __builtin_amdgcn_s_sleep(8);
      W1(1);
      waitv<0>();
    }
    BAR();
    BAR();
    if (m >= 14) {
      return;
    }
    const char* oa = ws + WS_O + ((size_t)(g * 4 + r2 * 2 + (lw >> 1)) * 32 + (lw & 1) * 2) * 1024;
    const char* ha = ws + WS_H + (size_t)(g * 4 + r2 * 2 + (lw & 1)) * 112 * 1024;
    const char* oq = ws + WS_Q + (size_t)((lw & 1) * 64 + g * 4 + r2 * 2 + (lw >> 1)) * 2048;
    auto CA = [&](int i) { glds16x2_sc1(oq + ((i + cb) & 7) * (128 * 2048), l16, lds0 + L_CA + i * 8192 + lw * 2048); };
    auto EA = [&](int j) { glds16x4_sc1(ha + EMS(j) * 4096, l16, lds0 + L_EA(j) + (lw & 1) * 4096); };
    const char* ya = ws + WS_Y + (size_t)(g * 4 + lw) * 28 * 1024;
    bf16x8 pp[4];
    auto DA = [&](int j) { glds16x2_sc1(ya + j * 2048, l16, lds0 + L_AR + (j % 5) * 8192 + lw * 2048); };
    auto STL = [&](int) {
      const unsigned so = (unsigned)WS_ST2 + (g * 4 + lw) * 4096 + l16;
      gld16x2_sc1<0>(ws, so, pp[0], pp[1]); gld16x2_sc1<2048>(ws, so, pp[2], pp[3]);
    };
    auto STC = [&]() {
      asm volatile("" : "+v"(pp[0]), "+v"(pp[1]), "+v"(pp[2]), "+v"(pp[3]));
      const f32x4 p0 = __builtin_bit_cast(f32x4, pp[0]), p1 = __builtin_bit_cast(f32x4, pp[1]), p2 = __builtin_bit_cast(f32x4, pp[2]), p3 = __builtin_bit_cast(f32x4, pp[3]);
      const float mean = swap32_add(p0[0] + p0[2] + p1[0] + p1[2] + p2[0] + p2[2] + p3[0]) * (1.f / 14.f);
      const float d0 = p0[0] - mean, d1 = p0[2] - mean, d2 = p1[0] - mean, d3 = p1[2] - mean, d4 = p2[0] - mean, d5 = p2[2] - mean, d6 = p3[0] - mean;
      float q = (p0[1] + p0[3] + p1[1] + p1[3] + p2[1] + p2[3] + p3[1]) + 32.f * (d0 * d0 + d1 * d1 + d2 * d2 + d3 * d3 + d4 * d4 + d5 * d5 + d6 * d6);
      q = swap32_add(q);
      const float rs = -1.4426950408889634f * __builtin_amdgcn_rsqf(q * (1.f / D) + LN_EPS);
      *(LAS f32x2*)(sm + L_ST + lw * 512 + lane * 8) = (f32x2){rs, -(mean - p3[2]) * rs};
    };
    auto AB = [&](int) {
      const int ch = lane >> 2;
      glds16(ws + WS_MODB + (3 + (lw & 1)) * D * 2, ch * 64 + (lane & 3) * 16, lds0 + L_AB + (lw & 1) * 1024);
    };
#define SIGNAL(i) do { if (lane == 0) __hip_atomic_fetch_add((LAS unsigned*)(sm + L_CNT + 4 * (i)), 1u, __ATOMIC_RELAXED, __HIP_MEMORY_SCOPE_WORKGROUP); } while (0)
    unsigned pqv;
    auto PQ = [&](int p) { asm volatile("global_load_dword %0, %1, %2 sc1" : "=v"(pqv) : "v"((unsigned)WS_FLAGS + (p * 256 + g * 16 + m) * 128), "s"(ws) : "memory"); };
    auto PCHK = [&]() { asm volatile("" : "+v"(pqv)); return (unsigned)__builtin_amdgcn_readfirstlane(pqv); };
#define LWAIT(p) lds_wait4((LAS unsigned*)(sm + L_CNT + 8 * (p)), 4u)
#define POLL(p) do { poll_count(cnts + ((p) * 256 + g * 16 + m) * 32, (p) ? 14u : 16u); } while (0)
    POLL(0);
    CA(0);
    CA(1);
    CA(2);
    CA(3);
    waitv<6>();
    BAR();
    CA(4);
    CA(5);
    waitv<8>();
    BAR();
    CA(6);
    CA(7);
    AB(0);
    waitv<11>();
    BAR();
    waitv<9>();
    BAR();
    W1(2);
    waitv<11>();
    BAR();
    W1(6);
    waitv<13>();
    BAR();
    W1(3);
    waitv<15>();
    BAR();
    waitv<13>();
    BAR();
    W1(4);
    waitv<4>();
    SIGNAL(0);
    BAR();
    W1(5);
    BAR();
    waitv<0>();
    SIGNAL(1);
    POLL(1);
    DA(0);
    DA(1);
    DA(2);
    STL(0);
    DA(3);
    DA(4);
    waitv<12>();
    BAR();
    waitv<10>();
    BAR();
    waitv<8>();
    BAR();
    DA(5);
    waitv<4>();
    BAR();
    DA(6);
    EB4(0);
    EB4(1);
    waitv<8>();
    STC();
    BAR();
    DA(7);
    waitv<8>();
    BAR();
    DA(8);
    EB4(2);
    EB4(3);
    waitv<12>();
    BAR();
    DA(9);
    waitv<8>();
    BAR();
    DA(10);
    EB4(4);
    EB4(5);
    waitv<12>();
    BAR();
    DA(11);
    waitv<8>();
    BAR();
    DA(12);
    EB4(6);
    EB4(7);
    waitv<12>();
    BAR();
    DA(13);
    waitv<8>();
    BAR();
    EB4(8);
    EB4(9);
    waitv<10>();
    BAR();
    waitv<4>();
    BAR();
    EB4(10);
    EB4(11);
    BAR();
    waitv<0>();
    if (lw < 2) {
      LWAIT(2);
      POLL(2);
      EA(0);
      EA(1);
      EA(2);
      EA(3);
      EA(4);
      EA(5);
      EA(6);
      waitv<24>();
      BAR();
      waitv<20>();
      BAR();
      waitv<16>();
      BAR();
      EA(7);
      waitv<16>();
      BAR();
      EA(8);
      waitv<16>();
      BAR();
      EA(9);
      waitv<16>();
      BAR();
      EA(10);
      waitv<16>();
      BAR();
      EA(11);
      waitv<16>();
      BAR();
      EA(12);
      waitv<16>();
      BAR();
      EA(13);
      waitv<16>();
      BAR();
      EA(14);
      waitv<16>();
      BAR();
      EA(15);
      waitv<16>();
      BAR();
      EA(16);
      waitv<16>();
      BAR();
      EA(17);
      waitv<16>();
      BAR();
      EA(18);
      waitv<16>();
      BAR();
      EA(19);
      waitv<16>();
      BAR();
      EA(20);
      waitv<16>();
      BAR();
      EA(21);
      waitv<16>();
      BAR();
      EA(22);
      waitv<16>();
      BAR();
      EA(23);
      waitv<16>();
      BAR();
      EA(24);
      waitv<16>();
      BAR();
      EA(25);
      waitv<16>();
      BAR();
      EA(26);
      waitv<16>();
      BAR();
      EA(27);
      waitv<16>();
      BAR();
      waitv<12>();
      BAR();
      waitv<8>();
      BAR();
      waitv<4>();
      BAR();
      waitv<0>();
      BAR();
    } else {
      BAR();
      BAR();
      BAR();
      EB(12);
      BAR();
      BAR();
      EB(13);
      BAR();
      EB(14);
      BAR();
      PQ(3);
      EB(15);
      BAR();
      BAR();
      EB(16);
      waitv<8>();
      if (PCHK() < 14u) POLL(3);
      BAR();
      EB(17);
      BAR();
      EB(18);
      BAR();
      BAR();
      EB(19);
      BAR();
      EB(20);
      BAR();
      EB(21);
      waitv<24>();
      BAR();
      waitv<20>();
      BAR();
      EB(22);
      waitv<20>();
      BAR();
      EB(23);
      waitv<20>();
      BAR();
      EB(24);
      waitv<20>();
      BAR();
      waitv<16>();
      BAR();
      EB(25);
      waitv<16>();
      BAR();
      EB(26);
      waitv<16>();
      BAR();
      EB(27);
      waitv<16>();
      BAR();
      waitv<12>();
      BAR();
      waitv<8>();
      BAR();
      waitv<4>();
      BAR();
      waitv<0>();
      BAR();
    }
#undef POLL
    return;
  }
  const f32x16 zero = {};
  {
    const int qh = wid & 1, kh = wid >> 1;
    f32x16 o[2] = {};
    float mref[2];
    const char* qs0 = ws + WS_Q + (size_t)(head * 64 + qblk * 4 + qh * 2 + kh) * 2048;
    const char* qs1 = ws + WS_Q + (size_t)(head * 64 + qblk * 4 + qh * 2 + (kh ^ 1)) * 2048;
    const char* ks = ws + WS_K + (size_t)(head * 64 + kh * 32) * 2048;
    const char* vs = ws + WS_V + (size_t)(head * 64 + kh * 32) * 2048;
    {
      unsigned voff = lane * 16, nm0, nm1;
      asm volatile(ATTN_ASM
                   : [o0] "+v"(o[0]), [o1] "+v"(o[1]), [voff] "+v"(voff), [nm0] "=&v"(nm0), [nm1] "=&v"(nm1)
                   : [qb0] "s"(qs0), [qb1] "s"(qs1), [kb0] "s"(ks), [kb1] "s"(ks + 4096), [kb2] "s"(ks + 8192), [kb3] "s"(ks + 12288),
                     [vb0] "s"(vs), [vb1] "s"(vs + 4096), [vb2] "s"(vs + 8192), [vb3] "s"(vs + 12288)
                   : ATTN_CLOBBERS, "s40", "s41", "s42", "scc", "memory");
      mref[0] = -bflo(nm0); mref[1] = -bflo(nm1);
    }
    f32x2 chk[4] = {};
#pragma unroll
    for (int i = 0; i < 2; ++i)
#pragma unroll
      for (int r = 0; r < 8; ++r) chk[r & 3] = __builtin_elementwise_fma((f32x2){o[i][2 * r], o[i][2 * r + 1]}, (f32x2){0.f, 0.f}, chk[r & 3]);
    chk[0] += chk[1]; chk[2] += chk[3]; chk[0] += chk[2];
    const bool bad = !(chk[0][0] + chk[0][1] == 0.f) || (h && !(fmaxf(o[0][12], o[1][12]) < 7.9e28f));
    if (__any(bad)) {
      bf16x8 qf[2][2];
#pragma unroll
      for (int i = 0; i < 2; ++i) {
        qf[i][0] = *(const bf16x8*)((i ? qs1 : qs0) + lane * 16); qf[i][1] = *(const bf16x8*)((i ? qs1 : qs0) + 1024 + lane * 16);
        o[i] = zero; mref[i] = -INFINITY;
      }
#pragma unroll 1
      for (int kt = 0; kt < 32; ++kt) {
        const bf16x8 k0 = *(const bf16x8*)(ks + kt * 2048 + lane * 16), k1 = *(const bf16x8*)(ks + kt * 2048 + 1024 + lane * 16);
        const bf16x8 v0 = *(const bf16x8*)(vs + kt * 2048 + lane * 16), v1 = *(const bf16x8*)(vs + kt * 2048 + 1024 + lane * 16);
#pragma unroll
        for (int i = 0; i < 2; ++i) {
          f32x16 s = MFMA32(k0, qf[i][0], zero); s = MFMA32(k1, qf[i][1], s);
          float mx = s[0];
#pragma unroll
          for (int r = 1; r < 16; ++r) mx = fmaxf(mx, s[r]);
          mx = fmaxf(swap32_max(mx), mref[i]);
          const float al = __builtin_amdgcn_exp2f(mref[i] - mx);
          mref[i] = mx;
#pragma unroll
          for (int r = 0; r < 16; ++r) { s[r] = __builtin_amdgcn_exp2f(s[r] - mx); o[i][r] *= al; }
          o[i] = MFMA32(v0, pack8(s, 0), o[i]); o[i] = MFMA32(v1, pack8(s, 1), o[i]);
        }
      }
    }
    f32x16 keep = o[0];
#pragma unroll
    for (int gq = 0; gq < 4; ++gq)
      *(LAS f32x4*)(sm + L_CA + wid * 4096 + gq * 1024 + lane * 16) = (f32x4){o[1][4 * gq], o[1][4 * gq + 1], o[1][4 * gq + 2], o[1][4 * gq + 3]};
    LAS float* ml = (LAS float*)(sm + L_CA + 4 * 4096);
    ml[wid * 64 + lane] = mref[1];
    BAR();
    const int pw = wid ^ 2;
    const float mo = ml[pw * 64 + lane];
    f32x4 t[4];
#pragma unroll
    for (int gq = 0; gq < 4; ++gq) t[gq] = *(const LAS f32x4*)(sm + L_CA + pw * 4096 + gq * 1024 + lane * 16);
    SB();
    const float mm = fmaxf(mo, mref[0]);
    const float fk = __builtin_amdgcn_exp2f(mref[0] - mm), fo = __builtin_amdgcn_exp2f(mo - mm);
#pragma unroll
    for (int gq = 0; gq < 4; ++gq)
#pragma unroll
      for (int j = 0; j < 4; ++j) keep[4 * gq + j] = keep[4 * gq + j] * fk + t[gq][j] * fo;
    auto rr = __builtin_amdgcn_permlane32_swap(__float_as_uint(keep[12]), __float_as_uint(keep[12]), false, false);
    keep *= __builtin_amdgcn_rcpf(__uint_as_float(rr[1]));
    if (h) { keep[12] = 0; keep[13] = 0; keep[14] = 0; keep[15] = 0; }
    const unsigned op = (unsigned)WS_Q + (head * 64 + qblk * 4 + qh * 2 + kh) * 2048 + lane * 16;
    st16_sc1(ws, op, pack8(keep, 0)); st16_sc1(ws, op + 1024, pack8(keep, 1));
    WAITV(0);
    BAR();
    if (wid == 0) publish(cnts + (qblk * 16) * 32, lane);
  }
  if (m >= 14) { return; }
  const int rt = wid & 1, ct = wid >> 1;
  const int g1 = (lane >> 4) & 1, q4 = (lane >> 2) & 3, p4 = lane & 3;
  const int trow = g * 4 + r2 * 2 + rt;
  const int orow = trow * 32 + (lane & 31), ocol = cb * 64 + ct * 32 + 16 * h;
  const unsigned cmap = ct * 256 + q4 * 64 + (16 * (p4 & 1) + 8 * g1 + 4 * (p4 >> 1)) * 2;
  bf16x8 x1p[2];
  {
    u32x4 rv[2]; f32x4 gv[4], av[4];
    rv[0] = *(const u32x4*)(x + (size_t)orow * D + ocol); rv[1] = *(const u32x4*)(x + (size_t)orow * D + ocol + 8);
#pragma unroll
    for (int i = 0; i < 4; ++i) { gv[i] = *(const f32x4*)(mod + 2 * D + ocol + 4 * i); av[i] = *(const f32x4*)(mod + 4 * D + ocol + 4 * i); }
    const float mx = ((const f32x4*)(ws + WS_ST1))[orow][2];
    f32x16 acc = {};
    unsigned bo0 = L_WO + cmap + h * 512, bo3 = L_WO + cmap + (h ? 5 : 6) * 512, abase = L_CA + rt * 4096 + lane * 16;
    PIN(bo0); PIN(bo3); PIN(abase);
    bf16x8 fa[2][4], fb[2][4];
#pragma unroll
    for (int stg = 0; stg <= 8; ++stg) {
      if (stg < 8) BARX();
#pragma unroll
      for (int s = 0; s < 4; ++s) {
        if (stg < 8) {
          fa[stg & 1][s] = *(const LAS bf16x8*)(sm + abase + stg * 8192 + s * 1024);
          const unsigned hb = (stg * 2 + (s >> 1)) * 7 * 512;
          if ((s & 1) == 0) fb[stg & 1][s] = cat8(trread(sm + bo0 + hb), trread(sm + bo0 + hb + 1024));
          else fb[stg & 1][s] = cat8(trread(sm + bo0 + hb + 2048), trread(sm + bo3 + hb));
          SB();
        }
        if (stg > 0) { acc = MFMA32(fb[(stg - 1) & 1][s], fa[(stg - 1) & 1][s], acc); SB(); }
      }
    }
    f32x16 xv, yv;
#pragma unroll
    for (int r = 0; r < 16; ++r) {
      const unsigned wr_ = rv[r >> 3][(r >> 1) & 3];
      xv[r] = fmaf(gv[r >> 2][r & 3], acc[r], (r & 1) ? bfhi(wr_) : bflo(wr_));
    }
    x1p[0] = pack8(xv, 0); x1p[1] = pack8(xv, 1);
#pragma unroll
    for (int r = 0; r < 16; ++r) {
      const unsigned wr_ = __builtin_bit_cast(u32x4, x1p[r >> 3])[(r >> 1) & 3];
      xv[r] = (r & 1) ? bfhi(wr_) : bflo(wr_);
      yv[r] = (xv[r] - mx) * av[r >> 2][r & 3];
    }
    const unsigned yp = (unsigned)WS_Y + (trow * 28 + (cb * 2 + ct) * 2) * 1024 + lane * 16;
    st16_sc1(ws, yp, pack8(yv, 0)); st16_sc1(ws, yp + 1024, pack8(yv, 1));
    float s = 0;
#pragma unroll
    for (int r = 0; r < 16; ++r) s += xv[r];
    const float mean = swap32_add(s) * (1.f / 32.f);
    float qq = 0;
#pragma unroll
    for (int r = 0; r < 16; ++r) { const float d = xv[r] - mean; qq += d * d; }
    qq = swap32_add(qq);
    {
      const int T = cb * 2 + ct, t7 = T % 7;
      const unsigned sp = (unsigned)WS_ST2 + ((trow * 4 + (t7 >> 1)) * 64 + (T / 7) * 32) * 16 + (t7 & 1) * 8;
      if (h == 0 || t7 == 6) st8_sc1(ws, sp + (lane & 31) * 16 + h * 8, h ? (f32x2){mx, 0.f} : (f32x2){mean, qq});
    }
    BAR();
    WAITV(0);
    BAR();
    if (wid == 0) publish(cnts + (256 + g * 16) * 32, lane);
  }
  {
    const int wr = rt, wc = ct;
    unsigned bsl[2];
    bsl[0] = L_SB + 4 * h * 1024 + wc * 512 + q4 * 64 + (16 * g1 + 4 * p4) * 2; bsl[1] = bsl[0] + 65536;
    PIN(bsl[0]); PIN(bsl[1]);
#define BOFF(kk) ((((kk) >> 2) & 3) * 16384 + (8 * (((kk) >> 1) & 1) + 2 * ((kk) & 1)) * 1024)
    {
      f32x16 uu = {};
      unsigned vo = L_AB + ((lane & 1) ? 0 : 1024) + h * 32, ub0 = bsl[0] + wr * 256, ub1 = bsl[1] + wr * 256;
      PIN(vo); PIN(ub0); PIN(ub1);
      lds_wait((LAS unsigned*)(sm + L_CNT), 4u);
      bf16x8 fv[4], fw[4];
#pragma unroll
      for (int ki = 0; ki < 32; ++ki) {
        const int s = ki & 3, kk = ki < 16 ? ki : ki < 20 ? ki + 8 : ki - 4;
        if (ki == 20) lds_wait((LAS unsigned*)(sm + L_CNT + 4), 4u);
        if (ki >= 4) { uu = MFMA32(fw[s], fv[s], uu); SB(); }
        if (ki < 28) {
          lds_ptr bp = sm + (kk < 16 ? ub0 : ub1) + BOFF(kk);
          fv[s] = *(const LAS bf16x8*)(sm + vo + (kk >> 1) * 64 + (kk & 1) * 16); fw[s] = cat8(trread(bp), trread(bp + 1024));
          SB();
        }
      }
      if ((lane & 31) < 2) {
        const float sc = (lane & 1) ? -1.4426950408889634f : 1.f;
#pragma unroll
        for (int gq = 0; gq < 4; ++gq)
          *(LAS f32x4*)(sm + L_UT + (lane & 1) * 512 + (wc * 64 + wr * 32 + 8 * gq + 4 * h) * 4) =
              (f32x4){uu[4 * gq] * sc, uu[4 * gq + 1] * sc, uu[4 * gq + 2] * sc, uu[4 * gq + 3] * sc};
      }
    }
    f32x16 acc[2][2] = {};
    float rstd[2], nm[2];
    unsigned abase = L_AR + wr * 4096 + lane * 16;
    PIN(abase);
    bf16x8 fa[4][2], fb[4][2];
#pragma unroll
    for (int kk = 0; kk < 30; ++kk) {
      const int s = kk & 3, c = (kk - 2) & 3;
      if (kk < 28 && (kk & 1) == 0) BARX();
      if (kk == 28) BAR();
      lds_ptr ap = sm + abase + ((kk >> 1) % 5) * 8192 + (kk & 1) * 1024;
      lds_ptr bp = sm + bsl[(kk >> 4) & 1] + BOFF(kk);
#pragma unroll
      for (int j = 0; j < 4; ++j) {
        if (kk >= 2) { acc[j >> 1][j & 1] = MFMA32(fb[c][j & 1], fa[c][j >> 1], acc[j >> 1][j & 1]); SB(); }
        if (kk < 28) {
          if (j == 0) fa[s][0] = *(const LAS bf16x8*)(ap);
          if (j == 1) fb[s][0] = cat8(trread(bp), trread(bp + 1024));
          if (j == 2) fb[s][1] = cat8(trread(bp + 256), trread(bp + 1280));
          if (j == 3) fa[s][1] = *(const LAS bf16x8*)(ap + 2048);
          SB();
        }
      }
      if (kk == 25) {
#pragma unroll
        for (int mi = 0; mi < 2; ++mi) { const f32x2 t = *(const LAS f32x2*)(sm + L_ST + (wr * 2 + mi) * 512 + lane * 8); rstd[mi] = t[0]; nm[mi] = t[1]; }
      }
    }
    const unsigned hp = (unsigned)WS_H + ((g * 4 + wr * 2) * 112 + m * 8 + wc * 2) * 1024 + lane * 16;
    lds_ptr utp = sm + L_UT + wc * 256 + h * 16;
    silu_tile(acc[0][0], rstd[0], nm[0], utp, ws, hp);
    silu_tile(acc[1][0], rstd[1], nm[1], utp, ws, hp + 112 * 1024);
    silu_tile(acc[0][1], rstd[0], nm[0], utp + 128, ws, hp + 4096);
    waitv<2>();
    arrive_publish((LAS unsigned*)(sm + L_CNT + 16), cnts + (512 + g * 16) * 32, lane);
    silu_tile(acc[1][1], rstd[1], nm[1], utp + 128, ws, hp + 112 * 1024 + 4096);
    WAITV(0);
    arrive_publish((LAS unsigned*)(sm + L_CNT + 20), cnts + (768 + g * 16) * 32, lane);
  }
  {
    f32x4 gv[4];
    f32x16 acc = {};
    unsigned ab, bb[2];
    ab = 12 * 8192 + rt * 4096 + lane * 16;
    bb[0] = cmap + h * 512; bb[1] = 6 * 8192 + cmap + h * 512;
    PIN(ab); PIN(bb[0]); PIN(bb[1]);
    bf16x8 fa[2][4], fb[2][4];
#pragma unroll
    for (int j = 0; j <= 28; ++j) {
      if (j < 28) BARX();
      if (j == 8) {
#pragma unroll
        for (int i = 0; i < 4; ++i) gv[i] = *(const f32x4*)(mod + 5 * D + ocol + 4 * i);
      }
#pragma unroll
      for (int s = 0; s < 4; ++s) {
        if (j < 28) {
          fa[j & 1][s] = *(const LAS bf16x8*)(sm + ab + (j % 7) * 8192 + s * 1024);
          lds_ptr bp = sm + bb[(j % 12) / 6] + ((j % 12) % 6) * 8192 + s * 2048;
          fb[j & 1][s] = cat8(trread(bp), trread(bp + 1024));
          SB();
        }
        if (j > 0) { acc = MFMA32(fb[(j - 1) & 1][s], fa[(j - 1) & 1][s], acc); SB(); }
      }
    }
    acc *= -0.6931471805599453f;
    f32x16 xv;
#pragma unroll
    for (int r = 0; r < 16; ++r) {
      const unsigned wr_ = __builtin_bit_cast(u32x4, x1p[r >> 3])[(r >> 1) & 3];
      xv[r] = fmaf(gv[r >> 2][r & 3], acc[r], (r & 1) ? bfhi(wr_) : bflo(wr_));
    }
    u16* op = out + (size_t)orow * D + ocol;
    *(bf16x8*)op = pack8(xv, 0); *(bf16x8*)(op + 8) = pack8(xv, 1);
  }
}

extern "C" void kernel_launch(void* const* d_in, const int* in_sizes, int n_in, void* d_out, int out_size, void* d_ws, size_t ws_size,
                              hipStream_t stream) {
  const u16* x = (const u16*)d_in[0];
  const u16* wqkv = (const u16*)d_in[1];
  const u16* wo = (const u16*)d_in[2];
  const u16* w1 = (const u16*)d_in[4];
  const u16* w2 = (const u16*)d_in[5];
  const u16* c = (const u16*)d_in[6];
  const u16* wmod = (const u16*)d_in[7];
  char* ws = (char*)d_ws;
  (void)hipFuncSetAttribute((const void*)k1_qkv, hipFuncAttributeMaxDynamicSharedMemorySize, K1_LDS2);
  (void)hipFuncSetAttribute((const void*)kb_fused, hipFuncAttributeMaxDynamicSharedMemorySize, KB_LDS);
  hipLaunchKernelGGL(k0_mod_stats, dim3(256), dim3(256), 0, stream, x, c, wmod, wqkv, (float*)(ws + WS_MOD), (f32x4*)(ws + WS_ST1), wo, w1, w2);
  hipLaunchKernelGGL(k1_qkv, dim3(256), dim3(768), K1_LDS2, stream, x, wqkv, (const float*)(ws + WS_MOD), (f32x4*)(ws + WS_ST1), (u16*)(ws + WS_Q));
  hipLaunchKernelGGL(kb_fused, dim3(256), dim3(512), KB_LDS, stream, x, wo, w1, w2, ws, (u16*)d_out);
}
```
